```python
import torch

def _softmax(x, axis=-1):
    return torch.softmax(x, dim=axis)
def _gelu(x):
    return torch.nn.functional.gelu(x, approximate="tanh")
def _silu(x):
    return torch.nn.functional.silu(x)
def _sigmoid(x):
    return torch.sigmoid(x)
def _layernorm(x, axis=-1, eps=1e-5):
    m = x.mean(dim=axis, keepdim=True)
    v = x.var(dim=axis, keepdim=True, unbiased=False)
    return (x - m) / torch.sqrt(v + eps)
def _scatter_add(tgt, idx, src, axis=0):
    return tgt.clone().index_add_(axis, idx, src)
def _segment_sum(src, seg, n):
    out = torch.zeros((n,) + src.shape[1:], dtype=src.dtype)
    return out.index_add_(0, seg, src)
def _pad(x, pw):
    flat = tuple(v for pair in reversed(pw) for v in pair)
    return torch.nn.functional.pad(x, flat)

kernel_name = 'standalone__detection_seg_fpn_lateral__vision'

def setup_inputs():

    _w = (lambda a: (lambda t: t.to(torch.bfloat16) if t.is_floating_point() else t)(torch.as_tensor(a)))
    H = W = (384 // 2) * 2
    C = 96
    k = 3
    return {
        "c_fine": _w((lambda *s: torch.randn(*s, dtype=torch.bfloat16) * 0.05)(1, H, W, C)),
        "p_coarse": _w((lambda *s: torch.randn(*s, dtype=torch.bfloat16) * 0.05)(1, H // 2, W // 2, C)),
        "w_lat": _w((lambda *s: torch.randn(*s, dtype=torch.bfloat16) * 0.05)(C, C)),
        "w_out": _w((lambda *s: torch.randn(*s, dtype=torch.bfloat16) * 0.05)(k, k, C, C)),
    }

def reference(c_fine, p_coarse, w_lat, w_out):
    B, H, W, C = c_fine.shape
    k = 3
    lat = torch.einsum("bhwc,cd->bhwd", c_fine, w_lat)
    up = (lambda x, n, axis: torch.repeat_interleave(x, n, dim=axis))((lambda x, n, axis: torch.repeat_interleave(x, n, dim=axis))(p_coarse, 2, axis=1), 2, axis=2)
    m = lat + up
    pad = k // 2
    mp = _pad(m, ((0, 0), (pad, pad), (pad, pad), (0, 0)))
    o = None
    for di in range(k):
        for dj in range(k):
            t = torch.einsum("bhwc,cd->bhwd", mp[:, di:di + H, dj:dj + W, :], w_out[di, dj])
            o = t if o is None else o + t
    return o
```

```cpp
#include <hip/hip_runtime.h>
#include <hip/hip_bf16.h>

__global__ __launch_bounds__(256, 1) void fpn_lateral_conv_kernel(const void* __restrict__ c_fine, const void* __restrict__ p_coarse,
                                                                  const void* __restrict__ w_lat, const void* __restrict__ w_out,
                                                                  void* __restrict__ out, void* __restrict__ ws) {
  __shared__ __attribute__((aligned(16))) char smem[163840];
  const unsigned bid = blockIdx.x;
  const unsigned wid = __builtin_amdgcn_readfirstlane(threadIdx.x >> 6);
  const unsigned lbase = (unsigned)(size_t)smem;
  asm volatile(
    "s_getpc_b64 s[56:57]\n\t"
    ".LKSTART%=:\n\t"
    "s_mov_b32 s61, %5\n\t"
    "s_mov_b32 s60, %6\n\t"
    "v_mbcnt_lo_u32_b32 v144, -1, 0\n\t"
    "v_mbcnt_hi_u32_b32 v144, -1, v144\n\t"
    "s_mov_b64 s[36:37], %0\n\t"
    "s_and_b32 s37, s37, 0xffff\n\t"
    "s_mov_b32 s38, 0x1b00000\n\t"
    "s_mov_b32 s39, 0x20000\n\t"
    "s_mov_b64 s[40:41], %1\n\t"
    "s_and_b32 s41, s41, 0xffff\n\t"
    "s_mov_b32 s42, 0x6c0000\n\t"
    "s_mov_b32 s43, 0x20000\n\t"
    "s_mov_b64 s[44:45], %2\n\t"
    "s_and_b32 s45, s45, 0xffff\n\t"
    "s_mov_b32 s46, 0x4800\n\t"
    "s_mov_b32 s47, 0x20000\n\t"
    "s_mov_b64 s[48:49], %3\n\t"
    "s_and_b32 s49, s49, 0xffff\n\t"
    "s_mov_b32 s50, 0x28800\n\t"
    "s_mov_b32 s51, 0x20000\n\t"
    "s_mov_b64 s[52:53], %4\n\t"
    "s_and_b32 s53, s53, 0xffff\n\t"
    "s_mov_b32 s54, 0x1b00000\n\t"
    "s_mov_b32 s55, 0x20000\n\t"
    "s_and_b32 s57, s57, 0xffff\n\t"
    "s_mov_b32 s58, .LKEND%= - .LKSTART%=\n\t"
    "s_mov_b32 s59, 0x20000\n\t"
    "v_lshlrev_b32 v203, 4, v144\n\t"
    "s_lshr_b32 s35, s61, 3\n\t"
    "s_lshl_b32 s85, s60, 5\n\t"
    "s_add_u32 s85, s85, s35\n\t"
    "s_lshl_b32 s85, s85, 10\n\t"
    "buffer_load_dwordx4 v[0:3], v203, s[44:47], s85 offen offset:0\n\t"
    "buffer_load_dwordx4 v[4:7], v203, s[48:51], s85 offen offset:0\n\t"
    "s_add_u32 s86, s85, 0x20000\n\t"
    "buffer_load_dwordx4 v[8:11], v203, s[48:51], s86 offen offset:0\n\t"
    "buffer_load_dwordx4 v[12:15], v203, s[56:59], s85 offen offset:0\n\t"
    "s_and_b32 s85, s61, 7\n\t"
    "s_lshl_b32 s85, s85, 2\n\t"
    "s_lshr_b32 s63, s35, 3\n\t"
    "s_add_u32 s63, s63, s85\n\t"
    "s_and_b32 s62, s35, 7\n\t"
    "s_mul_i32 s64, s62, 48\n\t"
    "s_mul_i32 s65, s63, 12\n\t"
    "s_and_b32 s66, s60, 1\n\t"
    "s_lshr_b32 s67, s60, 1\n\t"
    "v_and_b32 v145, 15, v144\n\t"
    "v_and_b32 v146, 48, v144\n\t"
    "v_mov_b32 v210, 0x80000000\n\t"
    "s_lshl_b32 s86, s60, 4\n\t"
    "v_add_u32 v147, s86, v145\n\t"
    "s_sub_u32 s86, s64, 1\n\t"
    "v_add_u32 v208, s86, v147\n\t"
    "v_cmp_gt_u32 vcc, 50, v147\n\t"
    "s_nop 1\n\t"
    "v_cndmask_b32 v209, v210, v208, vcc\n\t"
    "v_cmp_gt_u32 vcc, 0x180, v209\n\t"
    "v_mul_u32_u24 v198, 0xc0, v208\n\t"
    "v_add_u32 v198, v198, v146\n\t"
    "v_cndmask_b32 v198, v210, v198, vcc\n\t"
    "v_lshrrev_b32 v208, 1, v208\n\t"
    "v_mul_u32_u24 v199, 0xc0, v208\n\t"
    "v_add_u32 v199, v199, v146\n\t"
    "v_cndmask_b32 v199, v210, v199, vcc\n\t"
    "s_mul_i32 s70, s65, 0x12000\n\t"
    "s_sub_u32 s68, s70, 0x12000\n\t"
    "s_add_u32 s69, s70, 0xd8000\n\t"
    "s_mul_i32 s73, s63, 0x36000\n\t"
    "s_sub_u32 s71, s73, 0x9000\n\t"
    "s_add_u32 s72, s73, 0x36000\n\t"
    "s_cmp_eq_u32 s63, 0\n\t"
    "s_cselect_b32 s68, 0x80000000, s68\n\t"
    "s_cselect_b32 s71, 0x80000000, s71\n\t"
    "s_cmp_eq_u32 s63, 31\n\t"
    "s_cselect_b32 s69, 0x80000000, s69\n\t"
    "s_cselect_b32 s72, 0x80000000, s72\n\t"
    "s_mul_i32 s83, s60, 0xc00\n\t"
    "s_mul_i32 s82, s67, 0x4830\n\t"
    "s_mul_i32 s85, s66, 0xc08\n\t"
    "s_add_u32 s82, s82, s85\n\t"
    "s_mul_i32 s63, s67, 0x1800\n\t"
    "s_mul_i32 s62, s66, 0x600\n\t"
    "s_add_u32 s63, s63, s62\n\t"
    "v_add_u32 v105, 0x600, v203\n\t"
    "v_cmp_gt_u32 vcc, 32, v144\n\t"
    "s_nop 1\n\t"
    "v_cndmask_b32 v104, v105, v203, vcc\n\t"
    "s_cmp_eq_u32 s67, 0\n\t"
    "s_cselect_b32 s75, -1.0, 1.0\n\t"
    "s_cselect_b32 s84, 0, 0x3020\n\t"
    "s_cselect_b32 s33, 0x3020, 0\n\t"
    "s_cselect_b64 s[56:57], s[44:45], s[48:49]\n\t"
    "s_cselect_b32 s58, s46, s50\n\t"
    "s_mov_b32 s35, 0xd800\n\t"
    "s_cselect_b32 s35, 0x1800, s35\n\t"
    "s_cselect_b32 s34, 0, 0x80008000\n\t"
    "s_add_u32 m0, s82, 0x1b120\n\t"
    "s_mul_i32 s86, s35, 0\n\t"
    "s_add_u32 s86, s86, s62\n\t"
    "buffer_load_dwordx4 v203, s[56:59], s86 offen offset:0 lds\n\t"
    "buffer_load_dwordx4 v104, s[56:59], s86 offen offset:1024 lds\n\t"
    "buffer_load_dwordx4 v105, s[56:59], s86 offen offset:2048 lds\n\t"
    "s_add_u32 m0, s82, 0x1c930\n\t"
    "s_mul_i32 s87, s35, 1\n\t"
    "s_add_u32 s87, s87, s62\n\t"
    "buffer_load_dwordx4 v203, s[56:59], s87 offen offset:0 lds\n\t"
    "buffer_load_dwordx4 v104, s[56:59], s87 offen offset:1024 lds\n\t"
    "buffer_load_dwordx4 v105, s[56:59], s87 offen offset:2048 lds\n\t"
    "s_add_u32 m0, s82, 0x1e140\n\t"
    "s_mul_i32 s86, s35, 2\n\t"
    "s_add_u32 s86, s86, s62\n\t"
    "buffer_load_dwordx4 v203, s[56:59], s86 offen offset:0 lds\n\t"
    "buffer_load_dwordx4 v104, s[56:59], s86 offen offset:1024 lds\n\t"
    "v_and_b32 v209, 6, v147\n\t"
    "v_lshlrev_b32 v209, 3, v209\n\t"
    "v_xor_b32 v209, v209, v146\n\t"
    "v_mul_u32_u24 v194, 0xc0, v147\n\t"
    "v_add_u32 v194, v194, v209\n\t"
    "v_mov_b32 v209, 0x28000\n\t"
    "v_cmp_gt_u32 vcc, 50, v147\n\t"
    "s_nop 1\n\t"
    "v_cndmask_b32 v194, v209, v194, vcc\n\t"
    "v_add_u32 v195, 0x9600, v194\n\t"
    "v_add_u32 v202, 0x1ec00, v203\n\t"
    "s_add_u32 s85, s83, 0x12c00\n\t"
    "v_add_u32 v196, s85, v203\n\t"
    "s_xor_b32 s85, s60, 2\n\t"
    "s_mul_i32 s85, s85, 0xc00\n\t"
    "s_add_u32 s85, s85, 0x12c00\n\t"
    "v_add_u32 v197, s85, v203\n\t"
    "s_lshl_b32 s85, s66, 6\n\t"
    "v_mul_u32_u24 v208, 0xc0, v145\n\t"
    "v_add_u32 v208, s85, v208\n\t"
    "v_add_u32 v200, v208, v146\n\t"
    "v_lshrrev_b32 v208, 3, v144\n\t"
    "s_lshl_b32 s85, s66, 3\n\t"
    "v_add_u32 v208, s85, v208\n\t"
    "v_and_b32 v209, 7, v144\n\t"
    "v_lshlrev_b32 v209, 3, v209\n\t"
    "v_mul_u32_u24 v201, 0xc0, v208\n\t"
    "v_add_u32 v201, v201, v209\n\t"
    "v_add_u32 v201, 0x80, v201\n\t"
    "s_lshl_b32 s85, s67, 10\n\t"
    "s_add_u32 s85, s85, 0x23400\n\t"
    "v_lshlrev_b32 v211, 2, v208\n\t"
    "v_and_b32 v211, 56, v211\n\t"
    "v_xor_b32 v209, v209, v211\n\t"
    "v_lshlrev_b32 v211, 6, v208\n\t"
    "v_add_u32 v211, v211, v209\n\t"
    "v_add_u32 v211, s85, v211\n\t"
    "s_lshl_b32 s76, s66, 3\n\t"
    "v_add_u32 v209, s76, v146\n\t"
    "v_lshlrev_b32 v210, 2, v145\n\t"
    "v_and_b32 v210, 56, v210\n\t"
    "v_xor_b32 v209, v209, v210\n\t"
    "v_lshlrev_b32 v210, 6, v145\n\t"
    "v_add_u32 v210, v210, v209\n\t"
    "v_add_u32 v210, s85, v210\n\t"
    "s_add_u32 s74, s65, s67\n\t"
    "s_mul_i32 s74, s74, 0x180\n\t"
    "s_add_u32 s74, s74, s64\n\t"
    "s_mul_i32 s74, s74, 0xc0\n\t"
    "v_lshrrev_b32 v208, 5, v144\n\t"
    "v_lshlrev_b32 v208, 3, v208\n\t"
    "v_lshrrev_b32 v209, 2, v144\n\t"
    "v_and_b32 v209, 3, v209\n\t"
    "v_add_u32 v208, v208, v209\n\t"
    "v_mul_u32_u24 v208, 0xc0, v208\n\t"
    "v_lshrrev_b32 v209, 4, v144\n\t"
    "v_and_b32 v209, 1, v209\n\t"
    "v_mul_u32_u24 v209, 0xc08, v209\n\t"
    "v_add_u32 v208, v208, v209\n\t"
    "v_and_b32 v209, 3, v144\n\t"
    "v_lshlrev_b32 v148, 4, v209\n\t"
    "v_add_u32 v208, v208, v148\n\t"
    "s_lshl_b32 s85, s66, 6\n\t"
    "v_add_u32 v204, s85, v208\n\t"
    "s_lshl_b32 s85, s66, 3\n\t"
    "s_add_u32 s85, s85, 0x80\n\t"
    "v_add_u32 v205, s85, v208\n\t"
    "v_add_u32 v206, 0x1b0a0, v205\n\t"
    "s_mul_i32 s85, s66, 0xc00\n\t"
    "v_add_u32 v207, s85, v202\n\t"
    "v_lshrrev_b32 v208, 2, v145\n\t"
    "v_lshlrev_b32 v208, 4, v208\n\t"
    "v_cmp_eq_u32 vcc, v208, v146\n\t"
    "v_and_b32 v209, 1, v145\n\t"
    "v_lshlrev_b32 v209, 4, v209\n\t"
    "v_mov_b32 v148, 0x3f00\n\t"
    "v_lshlrev_b32 v148, v209, v148\n\t"
    "v_mov_b32 v149, 0\n\t"
    "v_cndmask_b32 v148, v149, v148, vcc\n\t"
    "v_and_b32 v209, 2, v145\n\t"
    "v_cmp_eq_u32 vcc, 0, v209\n\t"
    "s_nop 1\n\t"
    "v_cndmask_b32 v28, v149, v148, vcc\n\t"
    "v_cndmask_b32 v29, v148, v149, vcc\n\t"
    "v_mov_b32 v30, 0\n\t"
    "v_mov_b32 v31, 0\n\t"
    "v_mov_b32 v32, v28\n\t"
    "v_mov_b32 v33, v29\n\t"
    "v_lshlrev_b32 v208, 2, v28\n\t"
    "v_and_b32 v208, s34, v208\n\t"
    "v_or_b32 v34, v28, v208\n\t"
    "v_lshlrev_b32 v208, 2, v29\n\t"
    "v_and_b32 v208, s34, v208\n\t"
    "v_or_b32 v35, v29, v208\n\t"
    "v_lshrrev_b32 v208, 1, v28\n\t"
    "v_and_b32 v208, 0x800080, v208\n\t"
    "v_or_b32 v208, v28, v208\n\t"
    "v_accvgpr_write_b32 a164, v208\n\t"
    "v_accvgpr_write_b32 a168, v208\n\t"
    "v_lshrrev_b32 v208, 1, v29\n\t"
    "v_and_b32 v208, 0x800080, v208\n\t"
    "v_or_b32 v208, v29, v208\n\t"
    "v_accvgpr_write_b32 a165, v208\n\t"
    "v_accvgpr_write_b32 a169, v208\n\t"
    "v_accvgpr_write_b32 a166, 0\n\t"
    "v_accvgpr_write_b32 a167, 0\n\t"
    "buffer_load_dwordx4 v105, s[56:59], s86 offen offset:2048 lds\n\t"
    "s_add_u32 m0, s82, 0x120c0\n\t"
    "s_add_u32 s87, s63, 0x1800\n\t"
    "buffer_load_dwordx4 v203, s[48:51], s87 offen offset:0 lds\n\t"
    "buffer_load_dwordx4 v104, s[48:51], s87 offen offset:1024 lds\n\t"
    "buffer_load_dwordx4 v105, s[48:51], s87 offen offset:2048 lds\n\t"
    "s_add_u32 m0, s82, 0x138d0\n\t"
    "s_add_u32 s86, s63, 0xf000\n\t"
    "buffer_load_dwordx4 v203, s[48:51], s86 offen offset:0 lds\n\t"
    "buffer_load_dwordx4 v104, s[48:51], s86 offen offset:1024 lds\n\t"
    "buffer_load_dwordx4 v105, s[48:51], s86 offen offset:2048 lds\n\t"
    "s_add_u32 m0, s82, 0x150e0\n\t"
    "s_add_u32 s87, s63, 0x1c800\n\t"
    "buffer_load_dwordx4 v203, s[48:51], s87 offen offset:0 lds\n\t"
    "buffer_load_dwordx4 v104, s[48:51], s87 offen offset:1024 lds\n\t"
    "buffer_load_dwordx4 v105, s[48:51], s87 offen offset:2048 lds\n\t"
    "s_add_u32 m0, s82, 0\n\t"
    "s_add_u32 s86, s63, 0x4800\n\t"
    "buffer_load_dwordx4 v203, s[48:51], s86 offen offset:0 lds\n\t"
    "buffer_load_dwordx4 v104, s[48:51], s86 offen offset:1024 lds\n\t"
    "buffer_load_dwordx4 v105, s[48:51], s86 offen offset:2048 lds\n\t"
    "s_add_u32 m0, s82, 0x1810\n\t"
    "s_add_u32 s87, s63, 0x12000\n\t"
    "s_waitcnt vmcnt(12)\n\t"
    "s_barrier\n\t"
    "ds_read_b64_tr_b16 v[56:57], v206 offset:0\n\t"
    "ds_read_b64_tr_b16 v[58:59], v206 offset:768\n\t"
    "ds_read_b64_tr_b16 v[60:61], v206 offset:6160\n\t"
    "ds_read_b64_tr_b16 v[62:63], v206 offset:6928\n\t"
    "ds_read_b64_tr_b16 v[64:65], v206 offset:12320\n\t"
    "ds_read_b64_tr_b16 v[66:67], v206 offset:13088\n\t"
    "ds_read_b64_tr_b16 v[68:69], v206 offset:64\n\t"
    "buffer_load_dwordx4 v203, s[48:51], s87 offen offset:0 lds\n\t"
    "ds_read_b64_tr_b16 v[70:71], v206 offset:832\n\t"
    "ds_read_b64_tr_b16 v[72:73], v206 offset:6224\n\t"
    "ds_read_b64_tr_b16 v[74:75], v206 offset:6992\n\t"
    "ds_read_b64_tr_b16 v[76:77], v206 offset:12384\n\t"
    "ds_read_b64_tr_b16 v[78:79], v206 offset:13152\n\t"
    "ds_read_b64_tr_b16 v[80:81], v206 offset:128\n\t"
    "ds_read_b64_tr_b16 v[82:83], v206 offset:896\n\t"
    "ds_read_b64_tr_b16 v[84:85], v206 offset:6288\n\t"
    "buffer_load_dwordx4 v104, s[48:51], s87 offen offset:1024 lds\n\t"
    "ds_read_b64_tr_b16 v[86:87], v206 offset:7056\n\t"
    "ds_read_b64_tr_b16 v[88:89], v206 offset:12448\n\t"
    "ds_read_b64_tr_b16 v[90:91], v206 offset:13216\n\t"
    "s_add_u32 s85, s84, 0x1f950\n\t"
    "s_add_u32 s34, s33, 0x1f950\n\t"
    "v_add_u32 v188, s85, v204\n\t"
    "v_add_u32 v189, s85, v205\n\t"
    "v_add_u32 v190, s34, v204\n\t"
    "v_add_u32 v191, s34, v205\n\t"
    "v_add_u32 v192, 0x21160, v204\n\t"
    "v_add_u32 v193, 0x21160, v205\n\t"
    "ds_read_b64_tr_b16 a[172:173], v190 offset:0\n\t"
    "ds_read_b64_tr_b16 a[174:175], v192 offset:0\n\t"
    "buffer_load_dwordx4 v105, s[48:51], s87 offen offset:2048 lds\n\t"
    "s_add_u32 m0, s82, 0x3020\n\t"
    "s_add_u32 s86, s63, 0x1f800\n\t"
    "ds_read_b64_tr_b16 a[176:177], v190 offset:768\n\t"
    "ds_read_b64_tr_b16 a[178:179], v192 offset:768\n\t"
    "ds_read_b64_tr_b16 a[0:1], v188 offset:0\n\t"
    "ds_read_b64_tr_b16 a[2:3], v188 offset:768\n\t"
    "ds_read_b64_tr_b16 a[180:181], v190 offset:8\n\t"
    "ds_read_b64_tr_b16 a[182:183], v192 offset:8\n\t"
    "ds_read_b64_tr_b16 a[184:185], v190 offset:776\n\t"
    "buffer_load_dwordx4 v203, s[48:51], s86 offen offset:0 lds\n\t"
    "ds_read_b64_tr_b16 a[186:187], v192 offset:776\n\t"
    "ds_read_b64_tr_b16 a[4:5], v188 offset:8\n\t"
    "ds_read_b64_tr_b16 a[6:7], v188 offset:776\n\t"
    "ds_read_b64_tr_b16 a[188:189], v191 offset:0\n\t"
    "ds_read_b64_tr_b16 a[190:191], v193 offset:0\n\t"
    "ds_read_b64_tr_b16 a[192:193], v191 offset:768\n\t"
    "ds_read_b64_tr_b16 a[194:195], v193 offset:768\n\t"
    "ds_read_b64_tr_b16 a[8:9], v189 offset:0\n\t"
    "buffer_load_dwordx4 v104, s[48:51], s86 offen offset:1024 lds\n\t"
    "ds_read_b64_tr_b16 a[10:11], v189 offset:768\n\t"
    "buffer_load_dwordx4 v105, s[48:51], s86 offen offset:2048 lds\n\t"
    "s_add_u32 m0, s82, 0x9060\n\t"
    "s_add_u32 s87, s63, 0x7800\n\t"
    "s_waitcnt lgkmcnt(12)\n\t"
    "v_mfma_f32_16x16x32_bf16 v[0:3], v[32:35], a[172:175], 0\n\t"
    "v_mfma_f32_16x16x32_bf16 v[4:7], v[32:35], a[176:179], 0\n\t"
    "v_mfma_f32_16x16x32_bf16 v[0:3], v[28:31], a[0:3], v[0:3]\n\t"
    "buffer_load_dwordx4 v203, s[48:51], s87 offen offset:0 lds\n\t"
    "v_mfma_f32_16x16x32_bf16 v[4:7], v[30:33], a[0:3], v[4:7]\n\t"
    "s_waitcnt lgkmcnt(6)\n\t"
    "v_mfma_f32_16x16x32_bf16 v[8:11], v[32:35], a[180:183], 0\n\t"
    "v_mfma_f32_16x16x32_bf16 v[12:15], v[32:35], a[184:187], 0\n\t"
    "v_mfma_f32_16x16x32_bf16 v[8:11], v[28:31], a[4:7], v[8:11]\n\t"
    "buffer_load_dwordx4 v104, s[48:51], s87 offen offset:1024 lds\n\t"
    "v_mfma_f32_16x16x32_bf16 v[12:15], v[30:33], a[4:7], v[12:15]\n\t"
    "s_waitcnt lgkmcnt(0)\n\t"
    "v_mfma_f32_16x16x32_bf16 v[16:19], v[32:35], a[188:191], 0\n\t"
    "v_cvt_pk_bf16_f32 v24, v0, v1\n\t"
    "v_cvt_pk_bf16_f32 v25, v2, v3\n\t"
    "v_mfma_f32_16x16x32_bf16 v[20:23], v[32:35], a[192:195], 0\n\t"
    "v_cvt_pk_bf16_f32 v26, v4, v5\n\t"
    "v_cvt_pk_bf16_f32 v27, v6, v7\n\t"
    "v_mfma_f32_16x16x32_bf16 v[16:19], v[28:31], a[8:11], v[16:19]\n\t"
    "buffer_load_dwordx4 v105, s[48:51], s87 offen offset:2048 lds\n\t"
    "s_add_u32 m0, s82, 0xa870\n\t"
    "s_add_u32 s86, s63, 0x15000\n\t"
    "v_mfma_f32_16x16x32_bf16 v[20:23], v[30:33], a[8:11], v[20:23]\n\t"
    "s_waitcnt vmcnt(12)\n\t"
    "s_barrier\n\t"
    "s_add_u32 s85, s84, 0x120c0\n\t"
    "s_add_u32 s34, s33, 0x120c0\n\t"
    "v_add_u32 v188, s85, v204\n\t"
    "v_add_u32 v189, s85, v205\n\t"
    "v_add_u32 v190, s34, v204\n\t"
    "v_add_u32 v191, s34, v205\n\t"
    "v_add_u32 v192, 0x138d0, v204\n\t"
    "v_add_u32 v193, 0x138d0, v205\n\t"
    "ds_read_b64_tr_b16 a[196:197], v190 offset:0\n\t"
    "ds_read_b64_tr_b16 a[198:199], v192 offset:0\n\t"
    "ds_read_b64_tr_b16 a[200:201], v190 offset:768\n\t"
    "buffer_load_dwordx4 v203, s[48:51], s86 offen offset:0 lds\n\t"
    "ds_read_b64_tr_b16 a[202:203], v192 offset:768\n\t"
    "ds_read_b64_tr_b16 a[12:13], v188 offset:0\n\t"
    "ds_read_b64_tr_b16 a[14:15], v188 offset:768\n\t"
    "ds_read_b64_tr_b16 a[172:173], v190 offset:8\n\t"
    "ds_read_b64_tr_b16 a[174:175], v192 offset:8\n\t"
    "ds_read_b64_tr_b16 a[176:177], v190 offset:776\n\t"
    "ds_read_b64_tr_b16 a[178:179], v192 offset:776\n\t"
    "ds_read_b64_tr_b16 a[16:17], v188 offset:8\n\t"
    "buffer_load_dwordx4 v104, s[48:51], s86 offen offset:1024 lds\n\t"
    "ds_read_b64_tr_b16 a[18:19], v188 offset:776\n\t"
    "ds_read_b64_tr_b16 a[180:181], v191 offset:0\n\t"
    "ds_read_b64_tr_b16 a[182:183], v193 offset:0\n\t"
    "ds_read_b64_tr_b16 a[184:185], v191 offset:768\n\t"
    "ds_read_b64_tr_b16 a[186:187], v193 offset:768\n\t"
    "ds_read_b64_tr_b16 a[20:21], v189 offset:0\n\t"
    "ds_read_b64_tr_b16 a[22:23], v189 offset:768\n\t"
    "buffer_load_dwordx4 v105, s[48:51], s86 offen offset:2048 lds\n\t"
    "s_add_u32 m0, s82, 0xc080\n\t"
    "s_add_u32 s87, s63, 0x22800\n\t"
    "ds_read_b64_tr_b16 a[188:189], v190 offset:18480\n\t"
    "ds_read_b64_tr_b16 a[190:191], v192 offset:18480\n\t"
    "ds_read_b64_tr_b16 a[192:193], v190 offset:19248\n\t"
    "ds_read_b64_tr_b16 a[194:195], v192 offset:19248\n\t"
    "ds_read_b64_tr_b16 a[24:25], v188 offset:18480\n\t"
    "ds_read_b64_tr_b16 a[26:27], v188 offset:19248\n\t"
    "v_accvgpr_write_b32 a108, v24\n\t"
    "v_accvgpr_write_b32 a109, v25\n\t"
    "v_accvgpr_write_b32 a110, v26\n\t"
    "buffer_load_dwordx4 v203, s[48:51], s87 offen offset:0 lds\n\t"
    "v_accvgpr_write_b32 a111, v27\n\t"
    "s_waitcnt lgkmcnt(15)\n\t"
    "v_mfma_f32_16x16x32_bf16 v[0:3], v[32:35], a[196:199], 0\n\t"
    "ds_read_b64_tr_b16 a[196:197], v190 offset:18488\n\t"
    "v_cvt_pk_bf16_f32 v24, v8, v9\n\t"
    "v_mfma_f32_16x16x32_bf16 v[4:7], v[32:35], a[200:203], 0\n\t"
    "ds_read_b64_tr_b16 a[198:199], v192 offset:18488\n\t"
    "v_cvt_pk_bf16_f32 v25, v10, v11\n\t"
    "v_mfma_f32_16x16x32_bf16 v[0:3], v[28:31], a[12:15], v[0:3]\n\t"
    "ds_read_b64_tr_b16 a[200:201], v190 offset:19256\n\t"
    "v_cvt_pk_bf16_f32 v26, v12, v13\n\t"
    "v_mfma_f32_16x16x32_bf16 v[4:7], v[30:33], a[12:15], v[4:7]\n\t"
    "buffer_load_dwordx4 v104, s[48:51], s87 offen offset:1024 lds\n\t"
    "s_waitcnt lgkmcnt(15)\n\t"
    "v_mfma_f32_16x16x32_bf16 v[8:11], v[32:35], a[172:175], 0\n\t"
    "ds_read_b64_tr_b16 a[202:203], v192 offset:19256\n\t"
    "ds_read_b64_tr_b16 a[28:29], v188 offset:18488\n\t"
    "ds_read_b64_tr_b16 a[30:31], v188 offset:19256\n\t"
    "ds_read_b64_tr_b16 a[172:173], v191 offset:18480\n\t"
    "ds_read_b64_tr_b16 a[174:175], v193 offset:18480\n\t"
    "v_cvt_pk_bf16_f32 v27, v14, v15\n\t"
    "v_mfma_f32_16x16x32_bf16 v[12:15], v[32:35], a[176:179], 0\n\t"
    "buffer_load_dwordx4 v105, s[48:51], s87 offen offset:2048 lds\n\t"
    "v_mfma_f32_16x16x32_bf16 v[8:11], v[28:31], a[16:19], v[8:11]\n\t"
    "ds_read_b64_tr_b16 a[176:177], v191 offset:19248\n\t"
    "v_accvgpr_write_b32 a112, v24\n\t"
    "v_mfma_f32_16x16x32_bf16 v[12:15], v[30:33], a[16:19], v[12:15]\n\t"
    "ds_read_b64_tr_b16 a[178:179], v193 offset:19248\n\t"
    "ds_read_b64_tr_b16 a[32:33], v189 offset:18480\n\t"
    "ds_read_b64_tr_b16 a[34:35], v189 offset:19248\n\t"
    "v_accvgpr_write_b32 a113, v25\n\t"
    "v_accvgpr_write_b32 a114, v26\n\t"
    "v_accvgpr_write_b32 a115, v27\n\t"
    "v_cvt_pk_bf16_f32 v212, v16, v17\n\t"
    "v_cvt_pk_bf16_f32 v213, v18, v19\n\t"
    "s_waitcnt lgkmcnt(15)\n\t"
    "v_mfma_f32_16x16x32_bf16 v[16:19], v[32:35], a[180:183], 0\n\t"
    "v_cvt_pk_bf16_f32 v214, v20, v21\n\t"
    "v_cvt_pk_bf16_f32 v215, v22, v23\n\t"
    "v_mfma_f32_16x16x32_bf16 v[20:23], v[32:35], a[184:187], 0\n\t"
    "v_cvt_pk_bf16_f32 v216, v0, v1\n\t"
    "v_cvt_pk_bf16_f32 v217, v2, v3\n\t"
    "v_mfma_f32_16x16x32_bf16 v[16:19], v[28:31], a[20:23], v[16:19]\n\t"
    "v_cvt_pk_bf16_f32 v218, v4, v5\n\t"
    "v_cvt_pk_bf16_f32 v219, v6, v7\n\t"
    "v_mfma_f32_16x16x32_bf16 v[20:23], v[30:33], a[20:23], v[20:23]\n\t"
    "buffer_load_dwordx4 a[208:211], v198, s[36:39], s70 offen offset:0\n\t"
    "s_waitcnt lgkmcnt(12)\n\t"
    "v_mfma_f32_16x16x32_bf16 v[0:3], v[32:35], a[188:191], 0\n\t"
    "v_cvt_pk_bf16_f32 v220, v8, v9\n\t"
    "v_cvt_pk_bf16_f32 v221, v10, v11\n\t"
    "v_mfma_f32_16x16x32_bf16 v[4:7], v[32:35], a[192:195], 0\n\t"
    "v_cvt_pk_bf16_f32 v222, v12, v13\n\t"
    "v_cvt_pk_bf16_f32 v223, v14, v15\n\t"
    "v_mfma_f32_16x16x32_bf16 v[0:3], v[28:31], a[24:27], v[0:3]\n\t"
    "v_cvt_pk_bf16_f32 v224, v16, v17\n\t"
    "v_cvt_pk_bf16_f32 v225, v18, v19\n\t"
    "v_mfma_f32_16x16x32_bf16 v[4:7], v[30:33], a[24:27], v[4:7]\n\t"
    "buffer_load_dwordx4 a[212:215], v198, s[36:39], s70 offen offset:64\n\t"
    "s_waitcnt lgkmcnt(6)\n\t"
    "v_mfma_f32_16x16x32_bf16 v[8:11], v[32:35], a[196:199], 0\n\t"
    "v_cvt_pk_bf16_f32 v226, v20, v21\n\t"
    "v_cvt_pk_bf16_f32 v227, v22, v23\n\t"
    "v_mfma_f32_16x16x32_bf16 v[12:15], v[32:35], a[200:203], 0\n\t"
    "v_cvt_pk_bf16_f32 v228, v0, v1\n\t"
    "s_waitcnt vmcnt(11) lgkmcnt(0)\n\t"
    "s_barrier\n\t"
    "s_add_u32 s85, s84, 0\n\t"
    "s_add_u32 s34, s33, 0\n\t"
    "v_mfma_f32_16x16x32_bf16 v[8:11], v[28:31], a[28:31], v[8:11]\n\t"
    "ds_write_b128 v207, v[56:59] offset:0\n\t"
    "v_mfma_f32_16x16x32_bf16 v[12:15], v[30:33], a[28:31], v[12:15]\n\t"
    "v_add_u32 v188, s85, v204\n\t"
    "v_add_u32 v189, s85, v205\n\t"
    "v_mfma_f32_16x16x32_bf16 v[16:19], v[32:35], a[172:175], 0\n\t"
    "v_add_u32 v190, s34, v204\n\t"
    "v_add_u32 v191, s34, v205\n\t"
    "v_mfma_f32_16x16x32_bf16 v[20:23], v[32:35], a[176:179], 0\n\t"
    "ds_write_b128 v207, v[60:63] offset:1024\n\t"
    "v_mfma_f32_16x16x32_bf16 v[16:19], v[28:31], a[32:35], v[16:19]\n\t"
    "buffer_load_dwordx4 a[216:219], v198, s[36:39], s70 offen offset:128\n\t"
    "s_add_u32 s78, s70, 0x12000\n\t"
    "v_mfma_f32_16x16x32_bf16 v[20:23], v[30:33], a[32:35], v[20:23]\n\t"
    "v_add_u32 v192, 0x1810, v204\n\t"
    "v_add_u32 v193, 0x1810, v205\n\t"
    "v_cvt_pk_bf16_f32 v229, v2, v3\n\t"
    "v_cvt_pk_bf16_f32 v230, v4, v5\n\t"
    "ds_write_b128 v207, v[64:67] offset:2048\n\t"
    "buffer_load_dwordx4 a[220:223], v198, s[36:39], s78 offen offset:0\n\t"
    "v_cvt_pk_bf16_f32 v231, v6, v7\n\t"
    "v_cvt_pk_bf16_f32 v232, v8, v9\n\t"
    "v_cvt_pk_bf16_f32 v233, v10, v11\n\t"
    "v_cvt_pk_bf16_f32 v234, v12, v13\n\t"
    "v_cvt_pk_bf16_f32 v235, v14, v15\n\t"
    "v_cvt_pk_bf16_f32 v236, v16, v17\n\t"
    "v_cvt_pk_bf16_f32 v237, v18, v19\n\t"
    "v_cvt_pk_bf16_f32 v238, v20, v21\n\t"
    "ds_write_b128 v207, v[68:71] offset:6144\n\t"
    "v_cvt_pk_bf16_f32 v239, v22, v23\n\t"
    "ds_write_b128 v207, v[72:75] offset:7168\n\t"
    "buffer_load_dwordx4 a[224:227], v198, s[36:39], s78 offen offset:64\n\t"
    "ds_write_b128 v207, v[76:79] offset:8192\n\t"
    "buffer_load_dwordx4 a[228:231], v198, s[36:39], s78 offen offset:128\n\t"
    "ds_write_b128 v207, v[80:83] offset:12288\n\t"
    "buffer_load_dwordx4 a[116:119], v199, s[40:43], s73 offen offset:0\n\t"
    "ds_write_b128 v207, v[84:87] offset:13312\n\t"
    "buffer_load_dwordx4 a[120:123], v199, s[40:43], s73 offen offset:64\n\t"
    "ds_write_b128 v207, v[88:91] offset:14336\n\t"
    "buffer_load_dwordx4 a[124:127], v199, s[40:43], s73 offen offset:128\n\t"
    "s_add_u32 m0, s82, 0x120c0\n\t"
    "s_add_u32 s86, s63, 0xa800\n\t"
    "ds_read_b64_tr_b16 a[180:181], v190 offset:0\n\t"
    "ds_read_b64_tr_b16 a[182:183], v192 offset:0\n\t"
    "ds_read_b64_tr_b16 a[184:185], v190 offset:768\n\t"
    "ds_read_b64_tr_b16 a[186:187], v192 offset:768\n\t"
    "ds_read_b64_tr_b16 a[36:37], v188 offset:0\n\t"
    "ds_read_b64_tr_b16 a[38:39], v188 offset:768\n\t"
    "ds_read_b64_tr_b16 a[188:189], v190 offset:8\n\t"
    "ds_read_b64_tr_b16 a[190:191], v192 offset:8\n\t"
    "ds_read_b64_tr_b16 a[192:193], v190 offset:776\n\t"
    "ds_read_b64_tr_b16 a[194:195], v192 offset:776\n\t"
    "ds_read_b64_tr_b16 a[40:41], v188 offset:8\n\t"
    "ds_read_b64_tr_b16 a[42:43], v188 offset:776\n\t"
    "ds_read_b64_tr_b16 a[196:197], v191 offset:0\n\t"
    "ds_read_b64_tr_b16 a[198:199], v193 offset:0\n\t"
    "ds_read_b64_tr_b16 a[200:201], v191 offset:768\n\t"
    "ds_read_b64_tr_b16 a[202:203], v193 offset:768\n\t"
    "ds_read_b64_tr_b16 a[44:45], v189 offset:0\n\t"
    "ds_read_b64_tr_b16 a[46:47], v189 offset:768\n\t"
    "ds_read_b64_tr_b16 a[172:173], v190 offset:18480\n\t"
    "ds_read_b64_tr_b16 a[174:175], v192 offset:18480\n\t"
    "ds_read_b64_tr_b16 a[176:177], v190 offset:19248\n\t"
    "ds_read_b64_tr_b16 a[178:179], v192 offset:19248\n\t"
    "ds_read_b64_tr_b16 a[48:49], v188 offset:18480\n\t"
    "ds_read_b64_tr_b16 a[50:51], v188 offset:19248\n\t"
    "s_waitcnt lgkmcnt(15)\n\t"
    "v_mfma_f32_16x16x32_bf16 v[0:3], v[32:35], a[180:183], 0\n\t"
    "ds_read_b64_tr_b16 a[180:181], v190 offset:18488\n\t"
    "v_mfma_f32_16x16x32_bf16 v[4:7], v[32:35], a[184:187], 0\n\t"
    "ds_read_b64_tr_b16 a[182:183], v192 offset:18488\n\t"
    "v_mfma_f32_16x16x32_bf16 v[0:3], v[28:31], a[36:39], v[0:3]\n\t"
    "ds_read_b64_tr_b16 a[184:185], v190 offset:19256\n\t"
    "v_mfma_f32_16x16x32_bf16 v[4:7], v[30:33], a[36:39], v[4:7]\n\t"
    "ds_read_b64_tr_b16 a[186:187], v192 offset:19256\n\t"
    "s_waitcnt lgkmcnt(15)\n\t"
    "v_mfma_f32_16x16x32_bf16 v[8:11], v[32:35], a[188:191], 0\n\t"
    "ds_read_b64_tr_b16 a[52:53], v188 offset:18488\n\t"
    "v_mfma_f32_16x16x32_bf16 v[12:15], v[32:35], a[192:195], 0\n\t"
    "ds_read_b64_tr_b16 a[54:55], v188 offset:19256\n\t"
    "v_mfma_f32_16x16x32_bf16 v[8:11], v[28:31], a[40:43], v[8:11]\n\t"
    "ds_read_b64_tr_b16 a[188:189], v191 offset:18480\n\t"
    "v_cvt_pk_bf16_f32 v240, v0, v1\n\t"
    "v_mfma_f32_16x16x32_bf16 v[12:15], v[30:33], a[40:43], v[12:15]\n\t"
    "ds_read_b64_tr_b16 a[190:191], v193 offset:18480\n\t"
    "v_cvt_pk_bf16_f32 v241, v2, v3\n\t"
    "s_waitcnt lgkmcnt(14)\n\t"
    "v_mfma_f32_16x16x32_bf16 v[16:19], v[32:35], a[196:199], 0\n\t"
    "ds_read_b64_tr_b16 a[192:193], v191 offset:19248\n\t"
    "v_cvt_pk_bf16_f32 v242, v4, v5\n\t"
    "v_mfma_f32_16x16x32_bf16 v[20:23], v[32:35], a[200:203], 0\n\t"
    "ds_read_b64_tr_b16 a[194:195], v193 offset:19248\n\t"
    "v_cvt_pk_bf16_f32 v243, v6, v7\n\t"
    "v_mfma_f32_16x16x32_bf16 v[16:19], v[28:31], a[44:47], v[16:19]\n\t"
    "ds_read_b64_tr_b16 a[56:57], v189 offset:18480\n\t"
    "v_cvt_pk_bf16_f32 v244, v8, v9\n\t"
    "v_mfma_f32_16x16x32_bf16 v[20:23], v[30:33], a[44:47], v[20:23]\n\t"
    "ds_read_b64_tr_b16 a[58:59], v189 offset:19248\n\t"
    "v_cvt_pk_bf16_f32 v245, v10, v11\n\t"
    "s_waitcnt lgkmcnt(12)\n\t"
    "v_mfma_f32_16x16x32_bf16 v[0:3], v[32:35], a[172:175], 0\n\t"
    "v_cvt_pk_bf16_f32 v246, v12, v13\n\t"
    "v_cvt_pk_bf16_f32 v247, v14, v15\n\t"
    "v_mfma_f32_16x16x32_bf16 v[4:7], v[32:35], a[176:179], 0\n\t"
    "v_cvt_pk_bf16_f32 v248, v16, v17\n\t"
    "v_cvt_pk_bf16_f32 v249, v18, v19\n\t"
    "v_mfma_f32_16x16x32_bf16 v[0:3], v[28:31], a[48:51], v[0:3]\n\t"
    "v_cvt_pk_bf16_f32 v250, v20, v21\n\t"
    "v_cvt_pk_bf16_f32 v251, v22, v23\n\t"
    "v_mfma_f32_16x16x32_bf16 v[4:7], v[30:33], a[48:51], v[4:7]\n\t"
    "s_waitcnt lgkmcnt(6)\n\t"
    "v_mfma_f32_16x16x32_bf16 v[8:11], v[32:35], a[180:183], 0\n\t"
    "v_mfma_f32_16x16x32_bf16 v[12:15], v[32:35], a[184:187], 0\n\t"
    "v_mfma_f32_16x16x32_bf16 v[8:11], v[28:31], a[52:55], v[8:11]\n\t"
    "v_mfma_f32_16x16x32_bf16 v[12:15], v[30:33], a[52:55], v[12:15]\n\t"
    "v_cvt_pk_bf16_f32 v252, v0, v1\n\t"
    "v_cvt_pk_bf16_f32 v253, v2, v3\n\t"
    "s_waitcnt lgkmcnt(0)\n\t"
    "v_mfma_f32_16x16x32_bf16 v[16:19], v[32:35], a[188:191], 0\n\t"
    "s_waitcnt vmcnt(9)\n\t"
    "s_barrier\n\t"
    "ds_read_b64_tr_b16 a[196:197], v190 offset:36960\n\t"
    "v_mfma_f32_16x16x32_bf16 v[20:23], v[32:35], a[192:195], 0\n\t"
    "ds_read_b64_tr_b16 a[198:199], v192 offset:36960\n\t"
    "v_cvt_pk_bf16_f32 v254, v4, v5\n\t"
    "v_mfma_f32_16x16x32_bf16 v[16:19], v[28:31], a[56:59], v[16:19]\n\t"
    "ds_read_b64_tr_b16 a[200:201], v190 offset:37728\n\t"
    "v_cvt_pk_bf16_f32 v255, v6, v7\n\t"
    "v_mfma_f32_16x16x32_bf16 v[20:23], v[30:33], a[56:59], v[20:23]\n\t"
    "ds_read_b64_tr_b16 a[202:203], v192 offset:37728\n\t"
    "ds_read_b64_tr_b16 a[60:61], v188 offset:36960\n\t"
    "ds_read_b64_tr_b16 a[62:63], v188 offset:37728\n\t"
    "ds_read_b64_tr_b16 a[172:173], v190 offset:36968\n\t"
    "ds_read_b64_tr_b16 a[174:175], v192 offset:36968\n\t"
    "ds_read_b64_tr_b16 a[176:177], v190 offset:37736\n\t"
    "ds_read_b64_tr_b16 a[178:179], v192 offset:37736\n\t"
    "ds_read_b64_tr_b16 a[64:65], v188 offset:36968\n\t"
    "ds_read_b64_tr_b16 a[66:67], v188 offset:37736\n\t"
    "ds_read_b64_tr_b16 a[180:181], v191 offset:36960\n\t"
    "ds_read_b64_tr_b16 a[182:183], v193 offset:36960\n\t"
    "ds_read_b64_tr_b16 a[184:185], v191 offset:37728\n\t"
    "ds_read_b64_tr_b16 a[186:187], v193 offset:37728\n\t"
    "ds_read_b64_tr_b16 a[68:69], v189 offset:36960\n\t"
    "buffer_load_dwordx4 v203, s[48:51], s86 offen offset:0 lds\n\t"
    "ds_read_b64_tr_b16 a[70:71], v189 offset:37728\n\t"
    "ds_read_b64_tr_b16 a[188:189], v190 offset:55440\n\t"
    "ds_read_b64_tr_b16 a[190:191], v192 offset:55440\n\t"
    "ds_read_b64_tr_b16 a[192:193], v190 offset:56208\n\t"
    "ds_read_b64_tr_b16 a[194:195], v192 offset:56208\n\t"
    "ds_read_b64_tr_b16 a[72:73], v188 offset:55440\n\t"
    "ds_read_b64_tr_b16 a[74:75], v188 offset:56208\n\t"
    "ds_read_b128 a[140:143], v202 offset:0\n\t"
    "ds_read_b128 a[144:147], v202 offset:1024\n\t"
    "ds_read_b128 a[148:151], v202 offset:2048\n\t"
    "v_cvt_pk_bf16_f32 v120, v8, v9\n\t"
    "s_waitcnt lgkmcnt(15)\n\t"
    "v_mfma_f32_16x16x32_bf16 v[0:3], v[32:35], a[196:199], 0\n\t"
    "ds_read_b64_tr_b16 a[196:197], v190 offset:55448\n\t"
    "v_cvt_pk_bf16_f32 v121, v10, v11\n\t"
    "v_mfma_f32_16x16x32_bf16 v[4:7], v[32:35], a[200:203], 0\n\t"
    "ds_read_b64_tr_b16 a[198:199], v192 offset:55448\n\t"
    "v_cvt_pk_bf16_f32 v122, v12, v13\n\t"
    "v_mfma_f32_16x16x32_bf16 v[0:3], v[28:31], a[60:63], v[0:3]\n\t"
    "ds_read_b64_tr_b16 a[200:201], v190 offset:56216\n\t"
    "v_cvt_pk_bf16_f32 v123, v14, v15\n\t"
    "v_mfma_f32_16x16x32_bf16 v[4:7], v[30:33], a[60:63], v[4:7]\n\t"
    "ds_read_b64_tr_b16 a[202:203], v192 offset:56216\n\t"
    "v_cvt_pk_bf16_f32 v124, v16, v17\n\t"
    "v_mfma_f32_16x16x32_bf16 v[8:11], v[32:35], a[172:175], 0\n\t"
    "buffer_load_dwordx4 v104, s[48:51], s86 offen offset:1024 lds\n\t"
    "v_mfma_f32_16x16x32_bf16 v[12:15], v[32:35], a[176:179], 0\n\t"
    "buffer_load_dwordx4 v105, s[48:51], s86 offen offset:2048 lds\n\t"
    "s_add_u32 m0, s82, 0x138d0\n\t"
    "s_add_u32 s87, s63, 0x18000\n\t"
    "v_mfma_f32_16x16x32_bf16 v[8:11], v[28:31], a[64:67], v[8:11]\n\t"
    "ds_read_b64_tr_b16 a[76:77], v188 offset:55448\n\t"
    "v_cvt_pk_bf16_f32 v125, v18, v19\n\t"
    "v_mfma_f32_16x16x32_bf16 v[12:15], v[30:33], a[64:67], v[12:15]\n\t"
    "ds_read_b64_tr_b16 a[78:79], v188 offset:56216\n\t"
    "v_cvt_pk_bf16_f32 v126, v20, v21\n\t"
    "s_waitcnt lgkmcnt(15)\n\t"
    "v_mfma_f32_16x16x32_bf16 v[16:19], v[32:35], a[180:183], 0\n\t"
    "ds_read_b64_tr_b16 a[172:173], v191 offset:55440\n\t"
    "ds_read_b64_tr_b16 a[174:175], v193 offset:55440\n\t"
    "ds_read_b64_tr_b16 a[176:177], v191 offset:56208\n\t"
    "ds_read_b64_tr_b16 a[178:179], v193 offset:56208\n\t"
    "ds_read_b64_tr_b16 a[80:81], v189 offset:55440\n\t"
    "ds_read_b64_tr_b16 a[82:83], v189 offset:56208\n\t"
    "ds_read_b128 a[152:155], v202 offset:3072\n\t"
    "ds_read_b128 a[156:159], v202 offset:4096\n\t"
    "ds_read_b128 a[160:163], v202 offset:5120\n\t"
    "v_cvt_pk_bf16_f32 v127, v22, v23\n\t"
    "v_mfma_f32_16x16x32_bf16 v[20:23], v[32:35], a[184:187], 0\n\t"
    "v_cvt_pk_bf16_f32 v128, v0, v1\n\t"
    "v_cvt_pk_bf16_f32 v129, v2, v3\n\t"
    "v_mfma_f32_16x16x32_bf16 v[16:19], v[28:31], a[68:71], v[16:19]\n\t"
    "buffer_load_dwordx4 v203, s[48:51], s87 offen offset:0 lds\n\t"
    "v_mfma_f32_16x16x32_bf16 v[20:23], v[30:33], a[68:71], v[20:23]\n\t"
    "v_cvt_pk_bf16_f32 v130, v4, v5\n\t"
    "v_cvt_pk_bf16_f32 v131, v6, v7\n\t"
    "s_waitcnt lgkmcnt(15)\n\t"
    "v_mfma_f32_16x16x32_bf16 v[0:3], v[32:35], a[188:191], 0\n\t"
    "v_cvt_pk_bf16_f32 v132, v8, v9\n\t"
    "v_cvt_pk_bf16_f32 v133, v10, v11\n\t"
    "v_mfma_f32_16x16x32_bf16 v[4:7], v[32:35], a[192:195], 0\n\t"
    "v_cvt_pk_bf16_f32 v134, v12, v13\n\t"
    "v_cvt_pk_bf16_f32 v135, v14, v15\n\t"
    "v_mfma_f32_16x16x32_bf16 v[0:3], v[28:31], a[72:75], v[0:3]\n\t"
    "v_cvt_pk_bf16_f32 v136, v16, v17\n\t"
    "v_cvt_pk_bf16_f32 v137, v18, v19\n\t"
    "v_mfma_f32_16x16x32_bf16 v[4:7], v[30:33], a[72:75], v[4:7]\n\t"
    "v_cvt_pk_bf16_f32 v138, v20, v21\n\t"
    "v_cvt_pk_bf16_f32 v139, v22, v23\n\t"
    "s_waitcnt lgkmcnt(9)\n\t"
    "v_mfma_f32_16x16x32_bf16 v[8:11], v[32:35], a[196:199], 0\n\t"
    "v_mfma_f32_16x16x32_bf16 v[12:15], v[32:35], a[200:203], 0\n\t"
    "v_mfma_f32_16x16x32_bf16 v[8:11], v[28:31], a[76:79], v[8:11]\n\t"
    "v_cvt_pk_bf16_f32 v140, v0, v1\n\t"
    "v_cvt_pk_bf16_f32 v141, v2, v3\n\t"
    "v_mfma_f32_16x16x32_bf16 v[12:15], v[30:33], a[76:79], v[12:15]\n\t"
    "v_cvt_pk_bf16_f32 v142, v4, v5\n\t"
    "v_cvt_pk_bf16_f32 v143, v6, v7\n\t"
    "s_waitcnt lgkmcnt(3)\n\t"
    "v_mfma_f32_16x16x32_bf16 v[16:19], v[32:35], a[172:175], 0\n\t"
    "buffer_load_dwordx4 v104, s[48:51], s87 offen offset:1024 lds\n\t"
    "v_mfma_f32_16x16x32_bf16 v[20:23], v[32:35], a[176:179], 0\n\t"
    "buffer_load_dwordx4 v105, s[48:51], s87 offen offset:2048 lds\n\t"
    "s_add_u32 m0, s82, 0x150e0\n\t"
    "s_add_u32 s86, s63, 0x25800\n\t"
    "v_mfma_f32_16x16x32_bf16 v[16:19], v[28:31], a[80:83], v[16:19]\n\t"
    "v_cvt_pk_bf16_f32 v164, v8, v9\n\t"
    "v_cvt_pk_bf16_f32 v165, v10, v11\n\t"
    "v_mfma_f32_16x16x32_bf16 v[20:23], v[30:33], a[80:83], v[20:23]\n\t"
    "v_cvt_pk_bf16_f32 v166, v12, v13\n\t"
    "v_cvt_pk_bf16_f32 v167, v14, v15\n\t"
    "s_nop 2\n\t"
    "v_cvt_pk_bf16_f32 v168, v16, v17\n\t"
    "v_cvt_pk_bf16_f32 v169, v18, v19\n\t"
    "buffer_load_dwordx4 v203, s[48:51], s86 offen offset:0 lds\n\t"
    "v_cvt_pk_bf16_f32 v170, v20, v21\n\t"
    "v_cvt_pk_bf16_f32 v171, v22, v23\n\t"
    "buffer_load_dwordx4 v104, s[48:51], s86 offen offset:1024 lds\n\t"
    "buffer_load_dwordx4 v105, s[48:51], s86 offen offset:2048 lds\n\t"
    "s_waitcnt vmcnt(11)\n\t"
    "v_mfma_f32_16x16x32_bf16 v[56:59], a[164:167], a[116:119], 0\n\t"
    "v_mfma_f32_16x16x32_bf16 v[56:59], a[140:143], a[208:211], v[56:59]\n\t"
    "v_mfma_f32_16x16x32_bf16 v[56:59], a[144:147], a[212:215], v[56:59]\n\t"
    "v_mfma_f32_16x16x32_bf16 v[56:59], a[148:151], a[216:219], v[56:59]\n\t"
    "v_mfma_f32_16x16x32_bf16 v[64:67], a[164:167], a[116:119], 0\n\t"
    "v_mfma_f32_16x16x32_bf16 v[64:67], a[140:143], a[220:223], v[64:67]\n\t"
    "ds_read_b128 a[140:143], v202 offset:6144\n\t"
    "v_mfma_f32_16x16x32_bf16 v[64:67], a[144:147], a[224:227], v[64:67]\n\t"
    "buffer_load_dwordx4 a[232:235], v198, s[36:39], s68 offen offset:0\n\t"
    "v_mfma_f32_16x16x32_bf16 v[64:67], a[148:151], a[228:231], v[64:67]\n\t"
    "ds_read_b128 a[144:147], v202 offset:7168\n\t"
    "v_mfma_f32_16x16x32_bf16 v[60:63], a[166:169], a[116:119], 0\n\t"
    "ds_read_b128 a[148:151], v202 offset:8192\n\t"
    "s_waitcnt lgkmcnt(3)\n\t"
    "v_mfma_f32_16x16x32_bf16 v[60:63], a[152:155], a[208:211], v[60:63]\n\t"
    "v_mfma_f32_16x16x32_bf16 v[60:63], a[156:159], a[212:215], v[60:63]\n\t"
    "v_mfma_f32_16x16x32_bf16 v[60:63], a[160:163], a[216:219], v[60:63]\n\t"
    "v_mfma_f32_16x16x32_bf16 v[68:71], a[166:169], a[116:119], 0\n\t"
    "buffer_load_dwordx4 a[236:239], v198, s[36:39], s68 offen offset:64\n\t"
    "v_mfma_f32_16x16x32_bf16 v[68:71], a[152:155], a[220:223], v[68:71]\n\t"
    "ds_read_b128 a[152:155], v202 offset:9216\n\t"
    "v_add_f32 v144, v56, v64\n\t"
    "v_mfma_f32_16x16x32_bf16 v[68:71], a[156:159], a[224:227], v[68:71]\n\t"
    "ds_read_b128 a[156:159], v202 offset:10240\n\t"
    "v_add_f32 v145, v57, v65\n\t"
    "v_mfma_f32_16x16x32_bf16 v[68:71], a[160:163], a[228:231], v[68:71]\n\t"
    "ds_read_b128 a[160:163], v202 offset:11264\n\t"
    "v_add_f32 v146, v58, v66\n\t"
    "s_waitcnt vmcnt(12)\n\t"
    "v_mfma_f32_16x16x32_bf16 v[72:75], a[164:167], a[120:123], 0\n\t"
    "v_add_f32 v147, v59, v67\n\t"
    "v_cvt_pk_bf16_f32 v152, v144, v145\n\t"
    "v_cvt_pk_bf16_f32 v153, v146, v147\n\t"
    "s_nop 0\n\t"
    "v_add_f32 v148, v60, v68\n\t"
    "s_waitcnt lgkmcnt(3)\n\t"
    "v_mfma_f32_16x16x32_bf16 v[72:75], a[140:143], a[208:211], v[72:75]\n\t"
    "buffer_load_dwordx4 a[240:243], v198, s[36:39], s68 offen offset:128\n\t"
    "v_mfma_f32_16x16x32_bf16 v[72:75], a[144:147], a[212:215], v[72:75]\n\t"
    "v_add_f32 v149, v61, v69\n\t"
    "v_add_f32 v150, v62, v70\n\t"
    "v_mfma_f32_16x16x32_bf16 v[72:75], a[148:151], a[216:219], v[72:75]\n\t"
    "v_add_f32 v151, v63, v71\n\t"
    "v_cvt_pk_bf16_f32 v154, v148, v149\n\t"
    "v_mfma_f32_16x16x32_bf16 v[80:83], a[164:167], a[120:123], 0\n\t"
    "v_cvt_pk_bf16_f32 v155, v150, v151\n\t"
    "v_mfma_f32_16x16x32_bf16 v[80:83], a[140:143], a[220:223], v[80:83]\n\t"
    "ds_write_b128 v194, v[152:155] offset:9600\n\t"
    "v_mfma_f32_16x16x32_bf16 v[80:83], a[144:147], a[224:227], v[80:83]\n\t"
    "buffer_load_dwordx4 v[36:39], v199, s[40:43], s71 offen offset:0\n\t"
    "v_mfma_f32_16x16x32_bf16 v[80:83], a[148:151], a[228:231], v[80:83]\n\t"
    "v_sub_f32 v144, v56, v64\n\t"
    "v_sub_f32 v145, v57, v65\n\t"
    "v_mfma_f32_16x16x32_bf16 v[76:79], a[166:169], a[120:123], 0\n\t"
    "buffer_load_dwordx4 v[40:43], v199, s[40:43], s71 offen offset:64\n\t"
    "s_waitcnt lgkmcnt(1)\n\t"
    "v_mfma_f32_16x16x32_bf16 v[76:79], a[152:155], a[208:211], v[76:79]\n\t"
    "ds_read_b128 a[140:143], v202 offset:12288\n\t"
    "v_sub_f32 v146, v58, v66\n\t"
    "v_mfma_f32_16x16x32_bf16 v[76:79], a[156:159], a[212:215], v[76:79]\n\t"
    "ds_read_b128 a[144:147], v202 offset:13312\n\t"
    "v_sub_f32 v147, v59, v67\n\t"
    "v_mfma_f32_16x16x32_bf16 v[76:79], a[160:163], a[216:219], v[76:79]\n\t"
    "ds_read_b128 a[148:151], v202 offset:14336\n\t"
    "v_cvt_pk_bf16_f32 v156, v144, v145\n\t"
    "v_mfma_f32_16x16x32_bf16 v[84:87], a[166:169], a[120:123], 0\n\t"
    "buffer_load_dwordx4 v[44:47], v199, s[40:43], s71 offen offset:128\n\t"
    "s_add_u32 s79, s70, 0x24000\n\t"
    "v_mfma_f32_16x16x32_bf16 v[84:87], a[152:155], a[220:223], v[84:87]\n\t"
    "ds_read_b128 a[152:155], v202 offset:15360\n\t"
    "v_cvt_pk_bf16_f32 v157, v146, v147\n\t"
    "v_mfma_f32_16x16x32_bf16 v[84:87], a[156:159], a[224:227], v[84:87]\n\t"
    "ds_read_b128 a[156:159], v202 offset:16384\n\t"
    "v_sub_f32 v148, v60, v68\n\t"
    "v_mfma_f32_16x16x32_bf16 v[84:87], a[160:163], a[228:231], v[84:87]\n\t"
    "ds_read_b128 a[160:163], v202 offset:17408\n\t"
    "v_sub_f32 v149, v61, v69\n\t"
    "s_waitcnt vmcnt(15)\n\t"
    "v_mfma_f32_16x16x32_bf16 v[88:91], a[164:167], a[124:127], 0\n\t"
    "buffer_load_dwordx4 a[244:247], v198, s[36:39], s79 offen offset:0\n\t"
    "v_sub_f32 v150, v62, v70\n\t"
    "v_sub_f32 v151, v63, v71\n\t"
    "v_cvt_pk_bf16_f32 v158, v148, v149\n\t"
    "v_cvt_pk_bf16_f32 v159, v150, v151\n\t"
    "ds_write_b128 v194, v[156:159] offset:19200\n\t"
    "v_add_f32 v144, v72, v80\n\t"
    "v_add_f32 v145, v73, v81\n\t"
    "v_add_f32 v146, v74, v82\n\t"
    "v_add_f32 v147, v75, v83\n\t"
    "v_cvt_pk_bf16_f32 v160, v144, v145\n\t"
    "v_cvt_pk_bf16_f32 v161, v146, v147\n\t"
    "v_add_f32 v148, v76, v84\n\t"
    "v_add_f32 v149, v77, v85\n\t"
    "v_add_f32 v150, v78, v86\n\t"
    "v_add_f32 v151, v79, v87\n\t"
    "v_cvt_pk_bf16_f32 v162, v148, v149\n\t"
    "v_cvt_pk_bf16_f32 v163, v150, v151\n\t"
    "s_waitcnt lgkmcnt(4)\n\t"
    "v_mfma_f32_16x16x32_bf16 v[88:91], a[140:143], a[208:211], v[88:91]\n\t"
    "ds_write_b128 v194, v[160:163] offset:9664\n\t"
    "v_mfma_f32_16x16x32_bf16 v[88:91], a[144:147], a[212:215], v[88:91]\n\t"
    "buffer_load_dwordx4 a[248:251], v198, s[36:39], s79 offen offset:64\n\t"
    "v_mfma_f32_16x16x32_bf16 v[88:91], a[148:151], a[216:219], v[88:91]\n\t"
    "v_sub_f32 v144, v72, v80\n\t"
    "v_sub_f32 v145, v73, v81\n\t"
    "v_mfma_f32_16x16x32_bf16 v[96:99], a[164:167], a[124:127], 0\n\t"
    "v_sub_f32 v146, v74, v82\n\t"
    "v_sub_f32 v147, v75, v83\n\t"
    "v_mfma_f32_16x16x32_bf16 v[96:99], a[140:143], a[220:223], v[96:99]\n\t"
    "ds_read_b128 a[140:143], v202 offset:0\n\t"
    "v_cvt_pk_bf16_f32 v152, v144, v145\n\t"
    "v_mfma_f32_16x16x32_bf16 v[96:99], a[144:147], a[224:227], v[96:99]\n\t"
    "buffer_load_dwordx4 a[252:255], v198, s[36:39], s79 offen offset:128\n\t"
    "s_add_u32 s80, s73, 0x9000\n\t"
    "v_mfma_f32_16x16x32_bf16 v[96:99], a[148:151], a[228:231], v[96:99]\n\t"
    "s_waitcnt vmcnt(9)\n\t"
    "s_barrier\n\t"
    "s_add_u32 s85, s84, 0x120c0\n\t"
    "s_add_u32 s34, s33, 0x120c0\n\t"
    "v_add_u32 v188, s85, v204\n\t"
    "v_mfma_f32_16x16x32_bf16 v[92:95], a[166:169], a[124:127], 0\n\t"
    "v_add_u32 v189, s85, v205\n\t"
    "v_add_u32 v190, s34, v204\n\t"
    "s_waitcnt lgkmcnt(3)\n\t"
    "v_mfma_f32_16x16x32_bf16 v[92:95], a[152:155], a[208:211], v[92:95]\n\t"
    "v_add_u32 v191, s34, v205\n\t"
    "v_add_u32 v192, 0x138d0, v204\n\t"
    "v_mfma_f32_16x16x32_bf16 v[92:95], a[156:159], a[212:215], v[92:95]\n\t"
    "v_add_u32 v193, 0x138d0, v205\n\t"
    "ds_read_b64_tr_b16 a[180:181], v190 offset:0\n\t"
    "v_mfma_f32_16x16x32_bf16 v[92:95], a[160:163], a[216:219], v[92:95]\n\t"
    "ds_read_b64_tr_b16 a[182:183], v192 offset:0\n\t"
    "v_cvt_pk_bf16_f32 v153, v146, v147\n\t"
    "v_mfma_f32_16x16x32_bf16 v[100:103], a[166:169], a[124:127], 0\n\t"
    "ds_read_b64_tr_b16 a[184:185], v190 offset:768\n\t"
    "v_sub_f32 v148, v76, v84\n\t"
    "v_mfma_f32_16x16x32_bf16 v[100:103], a[152:155], a[220:223], v[100:103]\n\t"
    "ds_read_b64_tr_b16 a[186:187], v192 offset:768\n\t"
    "v_sub_f32 v149, v77, v85\n\t"
    "v_mfma_f32_16x16x32_bf16 v[100:103], a[156:159], a[224:227], v[100:103]\n\t"
    "ds_read_b64_tr_b16 a[84:85], v188 offset:0\n\t"
    "v_sub_f32 v150, v78, v86\n\t"
    "v_mfma_f32_16x16x32_bf16 v[100:103], a[160:163], a[228:231], v[100:103]\n\t"
    "ds_read_b64_tr_b16 a[86:87], v188 offset:768\n\t"
    "ds_read_b64_tr_b16 a[188:189], v190 offset:8\n\t"
    "ds_read_b64_tr_b16 a[190:191], v192 offset:8\n\t"
    "ds_read_b64_tr_b16 a[192:193], v190 offset:776\n\t"
    "ds_read_b64_tr_b16 a[194:195], v192 offset:776\n\t"
    "ds_read_b64_tr_b16 a[88:89], v188 offset:8\n\t"
    "ds_read_b64_tr_b16 a[90:91], v188 offset:776\n\t"
    "ds_read_b64_tr_b16 a[196:197], v191 offset:0\n\t"
    "ds_read_b64_tr_b16 a[198:199], v193 offset:0\n\t"
    "ds_read_b64_tr_b16 a[200:201], v191 offset:768\n\t"
    "ds_read_b64_tr_b16 a[202:203], v193 offset:768\n\t"
    "ds_read_b64_tr_b16 a[92:93], v189 offset:0\n\t"
    "ds_read_b64_tr_b16 a[94:95], v189 offset:768\n\t"
    "ds_read_b64_tr_b16 a[172:173], v190 offset:18480\n\t"
    "ds_read_b64_tr_b16 a[174:175], v192 offset:18480\n\t"
    "ds_read_b64_tr_b16 a[176:177], v190 offset:19248\n\t"
    "ds_read_b64_tr_b16 a[178:179], v192 offset:19248\n\t"
    "ds_read_b64_tr_b16 a[96:97], v188 offset:18480\n\t"
    "ds_read_b64_tr_b16 a[98:99], v188 offset:19248\n\t"
    "ds_read_b128 a[144:147], v202 offset:1024\n\t"
    "ds_read_b128 a[148:151], v202 offset:2048\n\t"
    "ds_read_b128 a[152:155], v202 offset:3072\n\t"
    "ds_read_b128 a[156:159], v202 offset:4096\n\t"
    "s_waitcnt lgkmcnt(15)\n\t"
    "v_mfma_f32_16x16x32_bf16 v[0:3], v[32:35], a[180:183], 0\n\t"
    "ds_read_b64_tr_b16 a[180:181], v190 offset:18488\n\t"
    "v_sub_f32 v151, v79, v87\n\t"
    "v_mfma_f32_16x16x32_bf16 v[4:7], v[32:35], a[184:187], 0\n\t"
    "ds_read_b64_tr_b16 a[182:183], v192 offset:18488\n\t"
    "v_cvt_pk_bf16_f32 v154, v148, v149\n\t"
    "v_mfma_f32_16x16x32_bf16 v[0:3], v[28:31], a[84:87], v[0:3]\n\t"
    "ds_read_b64_tr_b16 a[184:185], v190 offset:19256\n\t"
    "v_cvt_pk_bf16_f32 v155, v150, v151\n\t"
    "v_mfma_f32_16x16x32_bf16 v[4:7], v[30:33], a[84:87], v[4:7]\n\t"
    "ds_write_b128 v194, v[152:155] offset:19264\n\t"
    "v_mfma_f32_16x16x32_bf16 v[8:11], v[32:35], a[188:191], 0\n\t"
    "v_add_f32 v144, v88, v96\n\t"
    "v_add_f32 v145, v89, v97\n\t"
    "v_mfma_f32_16x16x32_bf16 v[12:15], v[32:35], a[192:195], 0\n\t"
    "v_cvt_pk_bf16_f32 v172, v0, v1\n\t"
    "v_cvt_pk_bf16_f32 v173, v2, v3\n\t"
    "v_mfma_f32_16x16x32_bf16 v[8:11], v[28:31], a[88:91], v[8:11]\n\t"
    "ds_read_b64_tr_b16 a[186:187], v192 offset:19256\n\t"
    "v_cvt_pk_bf16_f32 v174, v4, v5\n\t"
    "v_mfma_f32_16x16x32_bf16 v[12:15], v[30:33], a[88:91], v[12:15]\n\t"
    "ds_read_b64_tr_b16 a[100:101], v188 offset:18488\n\t"
    "v_cvt_pk_bf16_f32 v175, v6, v7\n\t"
    "s_waitcnt lgkmcnt(15)\n\t"
    "v_mfma_f32_16x16x32_bf16 v[16:19], v[32:35], a[196:199], 0\n\t"
    "ds_read_b64_tr_b16 a[102:103], v188 offset:19256\n\t"
    "v_add_f32 v146, v90, v98\n\t"
    "v_mfma_f32_16x16x32_bf16 v[20:23], v[32:35], a[200:203], 0\n\t"
    "ds_read_b64_tr_b16 a[188:189], v191 offset:18480\n\t"
    "v_cvt_pk_bf16_f32 v176, v8, v9\n\t"
    "v_mfma_f32_16x16x32_bf16 v[16:19], v[28:31], a[92:95], v[16:19]\n\t"
    "ds_read_b64_tr_b16 a[190:191], v193 offset:18480\n\t"
    "v_cvt_pk_bf16_f32 v177, v10, v11\n\t"
    "v_mfma_f32_16x16x32_bf16 v[20:23], v[30:33], a[92:95], v[20:23]\n\t"
    "ds_read_b64_tr_b16 a[192:193], v191 offset:19248\n\t"
    "v_cvt_pk_bf16_f32 v178, v12, v13\n\t"
    "s_waitcnt lgkmcnt(14)\n\t"
    "v_mfma_f32_16x16x32_bf16 v[0:3], v[32:35], a[172:175], 0\n\t"
    "ds_read_b64_tr_b16 a[194:195], v193 offset:19248\n\t"
    "v_cvt_pk_bf16_f32 v179, v14, v15\n\t"
    "v_mfma_f32_16x16x32_bf16 v[4:7], v[32:35], a[176:179], 0\n\t"
    "ds_read_b64_tr_b16 a[104:105], v189 offset:18480\n\t"
    "v_cvt_pk_bf16_f32 v180, v16, v17\n\t"
    "v_mfma_f32_16x16x32_bf16 v[0:3], v[28:31], a[96:99], v[0:3]\n\t"
    "ds_read_b64_tr_b16 a[106:107], v189 offset:19248\n\t"
    "v_lshrrev_b32 v204, 4, v203\n\t"
    "v_mfma_f32_16x16x32_bf16 v[4:7], v[30:33], a[96:99], v[4:7]\n\t"
    "v_and_b32 v205, 15, v204\n\t"
    "v_and_b32 v207, 48, v204\n\t"
    "s_add_u32 s85, s67, 1\n\t"
    "s_mul_i32 s85, s85, 0x2580\n\t"
    "s_mul_i32 s34, s67, 0x7080\n\t"
    "v_add_u32 v206, 0, v205\n\t"
    "v_and_b32 v209, 6, v206\n\t"
    "v_lshlrev_b32 v209, 3, v209\n\t"
    "v_xor_b32 v209, v209, v207\n\t"
    "v_mul_u32_u24 v206, 0xc0, v206\n\t"
    "v_add_u32 v206, v206, v209\n\t"
    "v_add_u32 v188, s85, v206\n\t"
    "v_add_u32 v191, s34, v206\n\t"
    "v_add_u32 v206, 1, v205\n\t"
    "v_and_b32 v209, 6, v206\n\t"
    "v_lshlrev_b32 v209, 3, v209\n\t"
    "v_xor_b32 v209, v209, v207\n\t"
    "v_mul_u32_u24 v206, 0xc0, v206\n\t"
    "v_add_u32 v206, v206, v209\n\t"
    "v_add_u32 v189, s85, v206\n\t"
    "s_waitcnt lgkmcnt(6)\n\t"
    "v_mfma_f32_16x16x32_bf16 v[8:11], v[32:35], a[180:183], 0\n\t"
    "v_add_u32 v192, s34, v206\n\t"
    "v_add_u32 v206, 2, v205\n\t"
    "v_mfma_f32_16x16x32_bf16 v[12:15], v[32:35], a[184:187], 0\n\t"
    "v_and_b32 v209, 6, v206\n\t"
    "v_lshlrev_b32 v209, 3, v209\n\t"
    "v_mfma_f32_16x16x32_bf16 v[8:11], v[28:31], a[100:103], v[8:11]\n\t"
    "v_xor_b32 v209, v209, v207\n\t"
    "v_mul_u32_u24 v206, 0xc0, v206\n\t"
    "v_mfma_f32_16x16x32_bf16 v[12:15], v[30:33], a[100:103], v[12:15]\n\t"
    "v_add_u32 v206, v206, v209\n\t"
    "v_add_u32 v190, s85, v206\n\t"
    "v_add_u32 v193, s34, v206\n\t"
    "ds_read_b128 a[160:163], v202 offset:5120\n\t"
    "v_cvt_pk_bf16_f32 v181, v18, v19\n\t"
    "buffer_load_dwordx4 a[128:131], v199, s[40:43], s80 offen offset:0\n\t"
    "v_cvt_pk_bf16_f32 v182, v20, v21\n\t"
    "v_cvt_pk_bf16_f32 v183, v22, v23\n\t"
    "v_cvt_pk_bf16_f32 v184, v0, v1\n\t"
    "s_waitcnt lgkmcnt(1)\n\t"
    "v_mfma_f32_16x16x32_bf16 v[16:19], v[32:35], a[188:191], 0\n\t"
    "v_cvt_pk_bf16_f32 v185, v2, v3\n\t"
    "v_cvt_pk_bf16_f32 v186, v4, v5\n\t"
    "v_mfma_f32_16x16x32_bf16 v[20:23], v[32:35], a[192:195], 0\n\t"
    "v_cvt_pk_bf16_f32 v187, v6, v7\n\t"
    "v_cvt_pk_bf16_f32 v48, v8, v9\n\t"
    "v_mfma_f32_16x16x32_bf16 v[16:19], v[28:31], a[104:107], v[16:19]\n\t"
    "v_cvt_pk_bf16_f32 v49, v10, v11\n\t"
    "v_cvt_pk_bf16_f32 v50, v12, v13\n\t"
    "v_mfma_f32_16x16x32_bf16 v[20:23], v[30:33], a[104:107], v[20:23]\n\t"
    "v_cvt_pk_bf16_f32 v51, v14, v15\n\t"
    "v_add_f32 v147, v91, v99\n\t"
    "v_cvt_pk_bf16_f32 v156, v144, v145\n\t"
    "v_cvt_pk_bf16_f32 v157, v146, v147\n\t"
    "v_add_f32 v148, v92, v100\n\t"
    "v_cvt_pk_bf16_f32 v52, v16, v17\n\t"
    "v_cvt_pk_bf16_f32 v53, v18, v19\n\t"
    "v_add_f32 v149, v93, v101\n\t"
    "v_cvt_pk_bf16_f32 v54, v20, v21\n\t"
    "v_cvt_pk_bf16_f32 v55, v22, v23\n\t"
    "v_add_f32 v150, v94, v102\n\t"
    "v_add_f32 v151, v95, v103\n\t"
    "v_cvt_pk_bf16_f32 v158, v148, v149\n\t"
    "v_cvt_pk_bf16_f32 v159, v150, v151\n\t"
    "ds_write_b128 v194, v[156:159] offset:9728\n\t"
    "v_sub_f32 v144, v88, v96\n\t"
    "v_sub_f32 v145, v89, v97\n\t"
    "buffer_load_dwordx4 a[132:135], v199, s[40:43], s80 offen offset:64\n\t"
    "buffer_load_dwordx4 a[136:139], v199, s[40:43], s80 offen offset:128\n\t"
    "s_add_u32 s81, s73, 0x12000\n\t"
    "v_sub_f32 v146, v90, v98\n\t"
    "v_sub_f32 v147, v91, v99\n\t"
    "v_cvt_pk_bf16_f32 v160, v144, v145\n\t"
    "v_cvt_pk_bf16_f32 v161, v146, v147\n\t"
    "v_sub_f32 v148, v92, v100\n\t"
    "v_sub_f32 v149, v93, v101\n\t"
    "v_sub_f32 v150, v94, v102\n\t"
    "v_sub_f32 v151, v95, v103\n\t"
    "v_cvt_pk_bf16_f32 v162, v148, v149\n\t"
    "v_cvt_pk_bf16_f32 v163, v150, v151\n\t"
    "ds_write_b128 v194, v[160:163] offset:19328\n\t"
    "buffer_load_dwordx4 a[116:119], v199, s[40:43], s81 offen offset:0\n\t"
    "s_add_u32 s78, s73, 0x12000\n\t"
    "s_waitcnt lgkmcnt(0)\n\t"
    "s_barrier\n\t"
    "ds_read_b128 a[172:175], v188 offset:0\n\t"
    "ds_read_b128 a[176:179], v188 offset:64\n\t"
    "s_waitcnt vmcnt(9)\n\t"
    "v_mfma_f32_16x16x32_bf16 v[104:107], a[164:167], v[36:39], 0\n\t"
    "buffer_load_dwordx4 a[120:123], v199, s[40:43], s78 offen offset:64\n\t"
    "s_add_u32 s79, s73, 0x12000\n\t"
    "v_mfma_f32_16x16x32_bf16 v[104:107], a[140:143], a[232:235], v[104:107]\n\t"
    "ds_read_b128 a[180:183], v188 offset:128\n\t"
    "v_mfma_f32_16x16x32_bf16 v[104:107], a[144:147], a[236:239], v[104:107]\n\t"
    "ds_read_b128 a[184:187], v189 offset:0\n\t"
    "v_mfma_f32_16x16x32_bf16 v[104:107], a[148:151], a[240:243], v[104:107]\n\t"
    "ds_read_b128 a[188:191], v189 offset:64\n\t"
    "ds_read_b128 a[192:195], v189 offset:128\n\t"
    "ds_read_b128 a[196:199], v190 offset:0\n\t"
    "ds_read_b128 a[200:203], v190 offset:64\n\t"
    "ds_read_b128 a[204:207], v190 offset:128\n\t"
    "buffer_load_dwordx4 a[124:127], v199, s[40:43], s79 offen offset:128\n\t"
    "s_add_u32 s80, s70, 0x36000\n\t"
    "s_nop 0\n\t"
    "v_sub_f32 v144, v104, v64\n\t"
    "v_sub_f32 v145, v105, v65\n\t"
    "v_sub_f32 v146, v106, v66\n\t"
    "v_sub_f32 v147, v107, v67\n\t"
    "v_cvt_pk_bf16_f32 v152, v144, v145\n\t"
    "v_cvt_pk_bf16_f32 v153, v146, v147\n\t"
    "buffer_load_dwordx4 a[208:211], v198, s[36:39], s80 offen offset:0\n\t"
    "s_waitcnt lgkmcnt(6)\n\t"
    "v_mfma_f32_16x16x32_bf16 v[0:3], a[108:111], a[172:175], 0\n\t"
    "v_mfma_f32_16x16x32_bf16 v[0:3], v[216:219], a[176:179], v[0:3]\n\t"
    "v_mfma_f32_16x16x32_bf16 v[0:3], v[228:231], a[180:183], v[0:3]\n\t"
    "v_mfma_f32_16x16x32_bf16 v[4:7], a[112:115], a[172:175], 0\n\t"
    "v_mfma_f32_16x16x32_bf16 v[4:7], v[220:223], a[176:179], v[4:7]\n\t"
    "v_mfma_f32_16x16x32_bf16 v[4:7], v[232:235], a[180:183], v[4:7]\n\t"
    "v_mfma_f32_16x16x32_bf16 v[8:11], v[212:215], a[172:175], 0\n\t"
    "ds_read_b128 a[172:175], v188 offset:3072\n\t"
    "v_mfma_f32_16x16x32_bf16 v[8:11], v[224:227], a[176:179], v[8:11]\n\t"
    "ds_read_b128 a[176:179], v188 offset:3136\n\t"
    "v_mfma_f32_16x16x32_bf16 v[8:11], v[236:239], a[180:183], v[8:11]\n\t"
    "ds_read_b128 a[180:183], v188 offset:3200\n\t"
    "s_waitcnt lgkmcnt(6)\n\t"
    "v_mfma_f32_16x16x32_bf16 v[0:3], v[240:243], a[184:187], v[0:3]\n\t"
    "buffer_load_dwordx4 a[212:215], v198, s[36:39], s80 offen offset:64\n\t"
    "v_mfma_f32_16x16x32_bf16 v[0:3], v[252:255], a[188:191], v[0:3]\n\t"
    "v_mfma_f32_16x16x32_bf16 v[0:3], v[128:131], a[192:195], v[0:3]\n\t"
    "v_mfma_f32_16x16x32_bf16 v[4:7], v[244:247], a[184:187], v[4:7]\n\t"
    "v_mfma_f32_16x16x32_bf16 v[4:7], v[120:123], a[188:191], v[4:7]\n\t"
    "v_mfma_f32_16x16x32_bf16 v[4:7], v[132:135], a[192:195], v[4:7]\n\t"
    "v_mfma_f32_16x16x32_bf16 v[8:11], v[248:251], a[184:187], v[8:11]\n\t"
    "ds_read_b128 a[184:187], v189 offset:3072\n\t"
    "v_mfma_f32_16x16x32_bf16 v[8:11], v[124:127], a[188:191], v[8:11]\n\t"
    "ds_read_b128 a[188:191], v189 offset:3136\n\t"
    "v_mfma_f32_16x16x32_bf16 v[8:11], v[136:139], a[192:195], v[8:11]\n\t"
    "ds_read_b128 a[192:195], v189 offset:3200\n\t"
    "s_waitcnt lgkmcnt(6)\n\t"
    "v_mfma_f32_16x16x32_bf16 v[0:3], v[140:143], a[196:199], v[0:3]\n\t"
    "v_mfma_f32_16x16x32_bf16 v[0:3], v[172:175], a[200:203], v[0:3]\n\t"
    "buffer_load_dwordx4 a[216:219], v198, s[36:39], s80 offen offset:128\n\t"
    "s_add_u32 s81, s70, 0x48000\n\t"
    "v_mfma_f32_16x16x32_bf16 v[0:3], v[184:187], a[204:207], v[0:3]\n\t"
    "v_mfma_f32_16x16x32_bf16 v[4:7], v[164:167], a[196:199], v[4:7]\n\t"
    "v_mfma_f32_16x16x32_bf16 v[4:7], v[176:179], a[200:203], v[4:7]\n\t"
    "v_mfma_f32_16x16x32_bf16 v[4:7], v[48:51], a[204:207], v[4:7]\n\t"
    "v_mfma_f32_16x16x32_bf16 v[8:11], v[168:171], a[196:199], v[8:11]\n\t"
    "ds_read_b128 a[196:199], v190 offset:3072\n\t"
    "v_mfma_f32_16x16x32_bf16 v[8:11], v[180:183], a[200:203], v[8:11]\n\t"
    "ds_read_b128 a[200:203], v190 offset:3136\n\t"
    "v_mfma_f32_16x16x32_bf16 v[8:11], v[52:55], a[204:207], v[8:11]\n\t"
    "ds_write_b128 v196, v[0:3] offset:0\n\t"
    "s_waitcnt lgkmcnt(6)\n\t"
    "v_mfma_f32_16x16x32_bf16 v[12:15], a[108:111], a[172:175], 0\n\t"
    "v_mfma_f32_16x16x32_bf16 v[12:15], v[216:219], a[176:179], v[12:15]\n\t"
    "buffer_load_dwordx4 a[220:223], v198, s[36:39], s81 offen offset:0\n\t"
    "v_mfma_f32_16x16x32_bf16 v[12:15], v[228:231], a[180:183], v[12:15]\n\t"
    "ds_write_b128 v196, v[4:7] offset:1024\n\t"
    "v_mfma_f32_16x16x32_bf16 v[16:19], a[112:115], a[172:175], 0\n\t"
    "v_mfma_f32_16x16x32_bf16 v[16:19], v[220:223], a[176:179], v[16:19]\n\t"
    "v_mfma_f32_16x16x32_bf16 v[16:19], v[232:235], a[180:183], v[16:19]\n\t"
    "ds_write_b128 v196, v[8:11] offset:2048\n\t"
    "v_mfma_f32_16x16x32_bf16 v[20:23], v[212:215], a[172:175], 0\n\t"
    "v_mfma_f32_16x16x32_bf16 v[20:23], v[224:227], a[176:179], v[20:23]\n\t"
    "v_mfma_f32_16x16x32_bf16 v[20:23], v[236:239], a[180:183], v[20:23]\n\t"
    "ds_read_b128 a[204:207], v190 offset:3200\n\t"
    "s_waitcnt lgkmcnt(6)\n\t"
    "v_mfma_f32_16x16x32_bf16 v[12:15], v[240:243], a[184:187], v[12:15]\n\t"
    "ds_read_b128 a[172:175], v188 offset:6144\n\t"
    "v_mfma_f32_16x16x32_bf16 v[12:15], v[252:255], a[188:191], v[12:15]\n\t"
    "ds_read_b128 a[176:179], v188 offset:6208\n\t"
    "v_mfma_f32_16x16x32_bf16 v[12:15], v[128:131], a[192:195], v[12:15]\n\t"
    "ds_read_b128 a[180:183], v188 offset:6272\n\t"
    "v_mfma_f32_16x16x32_bf16 v[16:19], v[244:247], a[184:187], v[16:19]\n\t"
    "v_mfma_f32_16x16x32_bf16 v[16:19], v[120:123], a[188:191], v[16:19]\n\t"
    "v_mfma_f32_16x16x32_bf16 v[16:19], v[132:135], a[192:195], v[16:19]\n\t"
    "v_mfma_f32_16x16x32_bf16 v[20:23], v[248:251], a[184:187], v[20:23]\n\t"
    "ds_read_b128 a[184:187], v189 offset:6144\n\t"
    "v_mfma_f32_16x16x32_bf16 v[20:23], v[124:127], a[188:191], v[20:23]\n\t"
    "ds_read_b128 a[188:191], v189 offset:6208\n\t"
    "v_mfma_f32_16x16x32_bf16 v[20:23], v[136:139], a[192:195], v[20:23]\n\t"
    "ds_read_b128 a[192:195], v189 offset:6272\n\t"
    "s_waitcnt vmcnt(9)\n\t"
    "v_mfma_f32_16x16x32_bf16 v[112:115], a[164:167], a[128:131], 0\n\t"
    "v_mfma_f32_16x16x32_bf16 v[112:115], a[140:143], a[244:247], v[112:115]\n\t"
    "ds_read_b128 a[140:143], v202 offset:6144\n\t"
    "v_mfma_f32_16x16x32_bf16 v[112:115], a[144:147], a[248:251], v[112:115]\n\t"
    "ds_read_b128 a[144:147], v202 offset:7168\n\t"
    "v_mfma_f32_16x16x32_bf16 v[112:115], a[148:151], a[252:255], v[112:115]\n\t"
    "ds_read_b128 a[148:151], v202 offset:8192\n\t"
    "v_mfma_f32_16x16x32_bf16 v[108:111], a[166:169], v[36:39], 0\n\t"
    "v_mfma_f32_16x16x32_bf16 v[108:111], a[152:155], a[232:235], v[108:111]\n\t"
    "v_mfma_f32_16x16x32_bf16 v[108:111], a[156:159], a[236:239], v[108:111]\n\t"
    "v_mfma_f32_16x16x32_bf16 v[108:111], a[160:163], a[240:243], v[108:111]\n\t"
    "v_mfma_f32_16x16x32_bf16 v[116:119], a[166:169], a[128:131], 0\n\t"
    "v_mfma_f32_16x16x32_bf16 v[116:119], a[152:155], a[244:247], v[116:119]\n\t"
    "ds_read_b128 a[152:155], v202 offset:9216\n\t"
    "v_mfma_f32_16x16x32_bf16 v[116:119], a[156:159], a[248:251], v[116:119]\n\t"
    "buffer_load_dwordx4 a[224:227], v198, s[36:39], s81 offen offset:64\n\t"
    "v_mfma_f32_16x16x32_bf16 v[116:119], a[160:163], a[252:255], v[116:119]\n\t"
    "ds_read_b128 a[156:159], v202 offset:10240\n\t"
    "v_mfma_f32_16x16x32_bf16 v[104:107], a[164:167], v[40:43], 0\n\t"
    "ds_read_b128 a[160:163], v202 offset:11264\n\t"
    "v_sub_f32 v148, v108, v68\n\t"
    "s_waitcnt lgkmcnt(12)\n\t"
    "v_mfma_f32_16x16x32_bf16 v[12:15], v[140:143], a[196:199], v[12:15]\n\t"
    "v_sub_f32 v149, v109, v69\n\t"
    "v_sub_f32 v150, v110, v70\n\t"
    "v_mfma_f32_16x16x32_bf16 v[12:15], v[172:175], a[200:203], v[12:15]\n\t"
    "v_sub_f32 v151, v111, v71\n\t"
    "v_cvt_pk_bf16_f32 v154, v148, v149\n\t"
    "s_waitcnt lgkmcnt(3)\n\t"
    "v_mfma_f32_16x16x32_bf16 v[104:107], a[140:143], a[232:235], v[104:107]\n\t"
    "v_cvt_pk_bf16_f32 v155, v150, v151\n\t"
    "v_mfma_f32_16x16x32_bf16 v[104:107], a[144:147], a[236:239], v[104:107]\n\t"
    "ds_write_b128 v194, v[152:155] offset:0\n\t"
    "v_mfma_f32_16x16x32_bf16 v[104:107], a[148:151], a[240:243], v[104:107]\n\t"
    "buffer_load_dwordx4 a[228:231], v198, s[36:39], s81 offen offset:128\n\t"
    "s_add_u32 s78, s70, 0x5a000\n\t"
    "v_mfma_f32_16x16x32_bf16 v[12:15], v[184:187], a[204:207], v[12:15]\n\t"
    "v_sub_f32 v144, v112, v56\n\t"
    "v_sub_f32 v145, v113, v57\n\t"
    "v_mfma_f32_16x16x32_bf16 v[16:19], v[164:167], a[196:199], v[16:19]\n\t"
    "v_sub_f32 v146, v114, v58\n\t"
    "v_sub_f32 v147, v115, v59\n\t"
    "s_waitcnt vmcnt(10)\n\t"
    "v_mfma_f32_16x16x32_bf16 v[56:59], a[164:167], a[132:135], 0\n\t"
    "v_cvt_pk_bf16_f32 v156, v144, v145\n\t"
    "v_cvt_pk_bf16_f32 v157, v146, v147\n\t"
    "v_mfma_f32_16x16x32_bf16 v[56:59], a[140:143], a[244:247], v[56:59]\n\t"
    "ds_write_b128 v196, v[12:15] offset:12288\n\t"
    "v_mfma_f32_16x16x32_bf16 v[56:59], a[144:147], a[248:251], v[56:59]\n\t"
    "v_sub_f32 v148, v116, v60\n\t"
    "v_sub_f32 v149, v117, v61\n\t"
    "v_mfma_f32_16x16x32_bf16 v[56:59], a[148:151], a[252:255], v[56:59]\n\t"
    "v_sub_f32 v150, v118, v62\n\t"
    "v_sub_f32 v151, v119, v63\n\t"
    "v_mfma_f32_16x16x32_bf16 v[108:111], a[166:169], v[40:43], 0\n\t"
    "ds_read_b128 a[140:143], v202 offset:12288\n\t"
    "v_cvt_pk_bf16_f32 v158, v148, v149\n\t"
    "s_waitcnt lgkmcnt(3)\n\t"
    "v_mfma_f32_16x16x32_bf16 v[108:111], a[152:155], a[232:235], v[108:111]\n\t"
    "ds_read_b128 a[144:147], v202 offset:13312\n\t"
    "v_cvt_pk_bf16_f32 v159, v150, v151\n\t"
    "v_mfma_f32_16x16x32_bf16 v[108:111], a[156:159], a[236:239], v[108:111]\n\t"
    "ds_write_b128 v194, v[156:159] offset:28800\n\t"
    "v_mfma_f32_16x16x32_bf16 v[108:111], a[160:163], a[240:243], v[108:111]\n\t"
    "v_sub_f32 v144, v104, v80\n\t"
    "v_sub_f32 v145, v105, v81\n\t"
    "v_mfma_f32_16x16x32_bf16 v[60:63], a[166:169], a[132:135], 0\n\t"
    "v_sub_f32 v146, v106, v82\n\t"
    "v_sub_f32 v147, v107, v83\n\t"
    "v_mfma_f32_16x16x32_bf16 v[60:63], a[152:155], a[244:247], v[60:63]\n\t"
    "ds_read_b128 a[148:151], v202 offset:14336\n\t"
    "v_cvt_pk_bf16_f32 v160, v144, v145\n\t"
    "v_mfma_f32_16x16x32_bf16 v[60:63], a[156:159], a[248:251], v[60:63]\n\t"
    "ds_read_b128 a[152:155], v202 offset:15360\n\t"
    "v_cvt_pk_bf16_f32 v161, v146, v147\n\t"
    "v_mfma_f32_16x16x32_bf16 v[60:63], a[160:163], a[252:255], v[60:63]\n\t"
    "ds_read_b128 a[156:159], v202 offset:16384\n\t"
    "v_sub_f32 v148, v108, v84\n\t"
    "v_mfma_f32_16x16x32_bf16 v[104:107], a[164:167], v[44:47], 0\n\t"
    "ds_read_b128 a[160:163], v202 offset:17408\n\t"
    "v_sub_f32 v149, v109, v85\n\t"
    "v_mfma_f32_16x16x32_bf16 v[16:19], v[176:179], a[200:203], v[16:19]\n\t"
    "v_sub_f32 v150, v110, v86\n\t"
    "v_sub_f32 v151, v111, v87\n\t"
    "v_mfma_f32_16x16x32_bf16 v[16:19], v[48:51], a[204:207], v[16:19]\n\t"
    "v_cvt_pk_bf16_f32 v162, v148, v149\n\t"
    "v_cvt_pk_bf16_f32 v163, v150, v151\n\t"
    "v_mfma_f32_16x16x32_bf16 v[20:23], v[168:171], a[196:199], v[20:23]\n\t"
    "ds_write_b128 v194, v[160:163] offset:64\n\t"
    "v_mfma_f32_16x16x32_bf16 v[20:23], v[180:183], a[200:203], v[20:23]\n\t"
    "v_sub_f32 v144, v56, v72\n\t"
    "v_sub_f32 v145, v57, v73\n\t"
    "v_mfma_f32_16x16x32_bf16 v[20:23], v[52:55], a[204:207], v[20:23]\n\t"
    "v_sub_f32 v146, v58, v74\n\t"
    "v_sub_f32 v147, v59, v75\n\t"
    "v_mfma_f32_16x16x32_bf16 v[24:27], a[108:111], a[172:175], 0\n\t"
    "ds_write_b128 v196, v[16:19] offset:13312\n\t"
    "v_mfma_f32_16x16x32_bf16 v[24:27], v[216:219], a[176:179], v[24:27]\n\t"
    "v_cvt_pk_bf16_f32 v152, v144, v145\n\t"
    "v_cvt_pk_bf16_f32 v153, v146, v147\n\t"
    "s_waitcnt lgkmcnt(5)\n\t"
    "v_mfma_f32_16x16x32_bf16 v[104:107], a[140:143], a[232:235], v[104:107]\n\t"
    "v_sub_f32 v148, v60, v76\n\t"
    "v_sub_f32 v149, v61, v77\n\t"
    "v_mfma_f32_16x16x32_bf16 v[104:107], a[144:147], a[236:239], v[104:107]\n\t"
    "ds_write_b128 v196, v[20:23] offset:14336\n\t"
    "v_mfma_f32_16x16x32_bf16 v[104:107], a[148:151], a[240:243], v[104:107]\n\t"
    "v_sub_f32 v150, v62, v78\n\t"
    "v_sub_f32 v151, v63, v79\n\t"
    "s_waitcnt vmcnt(9)\n\t"
    "v_mfma_f32_16x16x32_bf16 v[72:75], a[164:167], a[136:139], 0\n\t"
    "v_cvt_pk_bf16_f32 v154, v148, v149\n\t"
    "v_cvt_pk_bf16_f32 v155, v150, v151\n\t"
    "v_mfma_f32_16x16x32_bf16 v[72:75], a[140:143], a[244:247], v[72:75]\n\t"
    "ds_write_b128 v194, v[152:155] offset:28864\n\t"
    "v_mfma_f32_16x16x32_bf16 v[72:75], a[144:147], a[248:251], v[72:75]\n\t"
    "v_sub_f32 v144, v104, v96\n\t"
    "v_sub_f32 v145, v105, v97\n\t"
    "v_mfma_f32_16x16x32_bf16 v[72:75], a[148:151], a[252:255], v[72:75]\n\t"
    "v_sub_f32 v146, v106, v98\n\t"
    "v_sub_f32 v147, v107, v99\n\t"
    "v_mfma_f32_16x16x32_bf16 v[108:111], a[166:169], v[44:47], 0\n\t"
    "ds_read_b128 a[196:199], v190 offset:6144\n\t"
    "v_cvt_pk_bf16_f32 v156, v144, v145\n\t"
    "s_waitcnt lgkmcnt(5)\n\t"
    "v_mfma_f32_16x16x32_bf16 v[108:111], a[152:155], a[232:235], v[108:111]\n\t"
    "buffer_load_dwordx4 a[232:235], v198, s[36:39], s78 offen offset:0\n\t"
    "v_mfma_f32_16x16x32_bf16 v[108:111], a[156:159], a[236:239], v[108:111]\n\t"
    "buffer_load_dwordx4 a[236:239], v198, s[36:39], s78 offen offset:64\n\t"
    "v_mfma_f32_16x16x32_bf16 v[108:111], a[160:163], a[240:243], v[108:111]\n\t"
    "buffer_load_dwordx4 a[240:243], v198, s[36:39], s78 offen offset:128\n\t"
    "s_add_u32 s79, s70, 0x6c000\n\t"
    "v_mfma_f32_16x16x32_bf16 v[76:79], a[166:169], a[136:139], 0\n\t"
    "ds_read_b128 a[200:203], v190 offset:6208\n\t"
    "v_cvt_pk_bf16_f32 v157, v146, v147\n\t"
    "v_mfma_f32_16x16x32_bf16 v[76:79], a[152:155], a[244:247], v[76:79]\n\t"
    "buffer_load_dwordx4 a[244:247], v198, s[36:39], s79 offen offset:0\n\t"
    "v_mfma_f32_16x16x32_bf16 v[76:79], a[156:159], a[248:251], v[76:79]\n\t"
    "ds_read_b128 a[204:207], v190 offset:6272\n\t"
    "v_sub_f32 v148, v108, v100\n\t"
    "v_mfma_f32_16x16x32_bf16 v[76:79], a[160:163], a[252:255], v[76:79]\n\t"
    "ds_read_b128 a[140:143], v202 offset:0\n\t"
    "v_sub_f32 v149, v109, v101\n\t"
    "v_mfma_f32_16x16x32_bf16 v[104:107], a[164:167], a[128:131], 0\n\t"
    "ds_read_b128 a[144:147], v202 offset:1024\n\t"
    "v_sub_f32 v150, v110, v102\n\t"
    "v_mfma_f32_16x16x32_bf16 v[24:27], v[228:231], a[180:183], v[24:27]\n\t"
    "ds_read_b128 a[148:151], v202 offset:2048\n\t"
    "v_sub_f32 v151, v111, v103\n\t"
    "v_mfma_f32_16x16x32_bf16 v[28:31], a[112:115], a[172:175], 0\n\t"
    "ds_read_b128 a[152:155], v202 offset:3072\n\t"
    "v_cvt_pk_bf16_f32 v158, v148, v149\n\t"
    "v_mfma_f32_16x16x32_bf16 v[28:31], v[220:223], a[176:179], v[28:31]\n\t"
    "ds_read_b128 a[156:159], v202 offset:4096\n\t"
    "v_cvt_pk_bf16_f32 v159, v150, v151\n\t"
    "v_mfma_f32_16x16x32_bf16 v[28:31], v[232:235], a[180:183], v[28:31]\n\t"
    "ds_write_b128 v194, v[156:159] offset:128\n\t"
    "v_mfma_f32_16x16x32_bf16 v[32:35], v[212:215], a[172:175], 0\n\t"
    "buffer_load_dwordx4 a[248:251], v198, s[36:39], s79 offen offset:64\n\t"
    "v_mfma_f32_16x16x32_bf16 v[32:35], v[224:227], a[176:179], v[32:35]\n\t"
    "v_sub_f32 v144, v72, v88\n\t"
    "v_sub_f32 v145, v73, v89\n\t"
    "v_mfma_f32_16x16x32_bf16 v[32:35], v[236:239], a[180:183], v[32:35]\n\t"
    "ds_read_b128 a[160:163], v202 offset:5120\n\t"
    "v_sub_f32 v146, v74, v90\n\t"
    "v_sub_f32 v147, v75, v91\n\t"
    "v_cvt_pk_bf16_f32 v160, v144, v145\n\t"
    "v_cvt_pk_bf16_f32 v161, v146, v147\n\t"
    "v_sub_f32 v148, v76, v92\n\t"
    "v_sub_f32 v149, v77, v93\n\t"
    "v_sub_f32 v150, v78, v94\n\t"
    "v_sub_f32 v151, v79, v95\n\t"
    "v_cvt_pk_bf16_f32 v162, v148, v149\n\t"
    "v_cvt_pk_bf16_f32 v163, v150, v151\n\t"
    "ds_write_b128 v194, v[160:163] offset:28928\n\t"
    "s_waitcnt vmcnt(8) lgkmcnt(5)\n\t"
    "v_mfma_f32_16x16x32_bf16 v[104:107], a[140:143], a[208:211], v[104:107]\n\t"
    "v_mfma_f32_16x16x32_bf16 v[104:107], a[144:147], a[212:215], v[104:107]\n\t"
    "buffer_load_dwordx4 a[252:255], v198, s[36:39], s79 offen offset:128\n\t"
    "s_add_u32 s80, s73, 0x1b000\n\t"
    "v_mfma_f32_16x16x32_bf16 v[104:107], a[148:151], a[216:219], v[104:107]\n\t"
    "v_mfma_f32_16x16x32_bf16 v[88:91], a[164:167], a[116:119], 0\n\t"
    "s_nop 6\n\t"
    "v_sub_f32 v144, v64, v104\n\t"
    "s_waitcnt lgkmcnt(0)\n\t"
    "s_barrier\n\t"
    "v_mfma_f32_16x16x32_bf16 v[24:27], v[240:243], a[184:187], v[24:27]\n\t"
    "ds_read_b128 a[172:175], v191 offset:0\n\t"
    "v_mfma_f32_16x16x32_bf16 v[24:27], v[252:255], a[188:191], v[24:27]\n\t"
    "ds_read_b128 a[176:179], v191 offset:64\n\t"
    "v_mfma_f32_16x16x32_bf16 v[24:27], v[128:131], a[192:195], v[24:27]\n\t"
    "ds_read_b128 a[180:183], v191 offset:128\n\t"
    "v_sub_f32 v145, v65, v105\n\t"
    "v_mfma_f32_16x16x32_bf16 v[28:31], v[244:247], a[184:187], v[28:31]\n\t"
    "v_sub_f32 v146, v66, v106\n\t"
    "v_sub_f32 v147, v67, v107\n\t"
    "v_mfma_f32_16x16x32_bf16 v[28:31], v[120:123], a[188:191], v[28:31]\n\t"
    "v_cvt_pk_bf16_f32 v152, v144, v145\n\t"
    "v_cvt_pk_bf16_f32 v153, v146, v147\n\t"
    "v_mfma_f32_16x16x32_bf16 v[28:31], v[132:135], a[192:195], v[28:31]\n\t"
    "v_mfma_f32_16x16x32_bf16 v[32:35], v[248:251], a[184:187], v[32:35]\n\t"
    "ds_read_b128 a[184:187], v192 offset:0\n\t"
    "v_mfma_f32_16x16x32_bf16 v[32:35], v[124:127], a[188:191], v[32:35]\n\t"
    "ds_read_b128 a[188:191], v192 offset:64\n\t"
    "v_mfma_f32_16x16x32_bf16 v[32:35], v[136:139], a[192:195], v[32:35]\n\t"
    "ds_read_b128 a[192:195], v192 offset:128\n\t"
    "v_mfma_f32_16x16x32_bf16 v[24:27], v[140:143], a[196:199], v[24:27]\n\t"
    "v_mfma_f32_16x16x32_bf16 v[24:27], v[172:175], a[200:203], v[24:27]\n\t"
    "v_mfma_f32_16x16x32_bf16 v[24:27], v[184:187], a[204:207], v[24:27]\n\t"
    "v_mfma_f32_16x16x32_bf16 v[28:31], v[164:167], a[196:199], v[28:31]\n\t"
    "v_mfma_f32_16x16x32_bf16 v[28:31], v[176:179], a[200:203], v[28:31]\n\t"
    "v_mfma_f32_16x16x32_bf16 v[28:31], v[48:51], a[204:207], v[28:31]\n\t"
    "v_mfma_f32_16x16x32_bf16 v[32:35], v[168:171], a[196:199], v[32:35]\n\t"
    "ds_read_b128 a[196:199], v193 offset:0\n\t"
    "v_mfma_f32_16x16x32_bf16 v[32:35], v[180:183], a[200:203], v[32:35]\n\t"
    "ds_read_b128 a[200:203], v193 offset:64\n\t"
    "v_mfma_f32_16x16x32_bf16 v[32:35], v[52:55], a[204:207], v[32:35]\n\t"
    "ds_write_b128 v196, v[24:27] offset:24576\n\t"
    "s_waitcnt lgkmcnt(6)\n\t"
    "v_mfma_f32_16x16x32_bf16 v[0:3], a[0:3], a[172:175], v[0:3]\n\t"
    "v_mfma_f32_16x16x32_bf16 v[0:3], a[12:15], a[176:179], v[0:3]\n\t"
    "v_mfma_f32_16x16x32_bf16 v[0:3], a[24:27], a[180:183], v[0:3]\n\t"
    "ds_write_b128 v196, v[28:31] offset:25600\n\t"
    "v_mfma_f32_16x16x32_bf16 v[4:7], a[4:7], a[172:175], v[4:7]\n\t"
    "v_mfma_f32_16x16x32_bf16 v[4:7], a[16:19], a[176:179], v[4:7]\n\t"
    "v_mfma_f32_16x16x32_bf16 v[4:7], a[28:31], a[180:183], v[4:7]\n\t"
    "ds_write_b128 v196, v[32:35] offset:26624\n\t"
    "s_waitcnt vmcnt(6)\n\t"
    "v_mfma_f32_16x16x32_bf16 v[88:91], a[140:143], a[220:223], v[88:91]\n\t"
    "v_mfma_f32_16x16x32_bf16 v[88:91], a[144:147], a[224:227], v[88:91]\n\t"
    "v_mfma_f32_16x16x32_bf16 v[88:91], a[148:151], a[228:231], v[88:91]\n\t"
    "ds_read_b128 a[204:207], v193 offset:128\n\t"
    "v_mfma_f32_16x16x32_bf16 v[108:111], a[166:169], a[128:131], 0\n\t"
    "buffer_load_dwordx4 a[128:131], v199, s[40:43], s80 offen offset:0\n\t"
    "s_add_u32 s81, s73, 0x1b000\n\t"
    "v_mfma_f32_16x16x32_bf16 v[108:111], a[152:155], a[208:211], v[108:111]\n\t"
    "ds_read_b128 a[140:143], v202 offset:6144\n\t"
    "v_mfma_f32_16x16x32_bf16 v[108:111], a[156:159], a[212:215], v[108:111]\n\t"
    "ds_read_b128 a[144:147], v202 offset:7168\n\t"
    "v_mfma_f32_16x16x32_bf16 v[108:111], a[160:163], a[216:219], v[108:111]\n\t"
    "ds_read_b128 a[148:151], v202 offset:8192\n\t"
    "v_mfma_f32_16x16x32_bf16 v[92:95], a[166:169], a[116:119], 0\n\t"
    "v_mfma_f32_16x16x32_bf16 v[92:95], a[152:155], a[220:223], v[92:95]\n\t"
    "ds_read_b128 a[152:155], v202 offset:9216\n\t"
    "v_mfma_f32_16x16x32_bf16 v[92:95], a[156:159], a[224:227], v[92:95]\n\t"
    "ds_read_b128 a[156:159], v202 offset:10240\n\t"
    "v_mfma_f32_16x16x32_bf16 v[92:95], a[160:163], a[228:231], v[92:95]\n\t"
    "ds_read_b128 a[160:163], v202 offset:11264\n\t"
    "v_sub_f32 v148, v68, v108\n\t"
    "v_mfma_f32_16x16x32_bf16 v[64:67], a[164:167], a[132:135], 0\n\t"
    "v_sub_f32 v149, v69, v109\n\t"
    "v_sub_f32 v150, v70, v110\n\t"
    "v_mfma_f32_16x16x32_bf16 v[8:11], a[8:11], a[172:175], v[8:11]\n\t"
    "ds_read_b128 a[172:175], v191 offset:3072\n\t"
    "v_sub_f32 v151, v71, v111\n\t"
    "v_mfma_f32_16x16x32_bf16 v[8:11], a[20:23], a[176:179], v[8:11]\n\t"
    "ds_read_b128 a[176:179], v191 offset:3136\n\t"
    "v_cvt_pk_bf16_f32 v154, v148, v149\n\t"
    "v_mfma_f32_16x16x32_bf16 v[8:11], a[32:35], a[180:183], v[8:11]\n\t"
    "ds_read_b128 a[180:183], v191 offset:3200\n\t"
    "s_waitcnt lgkmcnt(10)\n\t"
    "s_barrier\n\t"
    "v_mfma_f32_16x16x32_bf16 v[0:3], a[36:39], a[184:187], v[0:3]\n\t"
    "ds_read_b128 v[36:39], v197 offset:0\n\t"
    "v_cvt_pk_bf16_f32 v155, v150, v151\n\t"
    "v_mfma_f32_16x16x32_bf16 v[0:3], a[48:51], a[188:191], v[0:3]\n\t"
    "ds_write_b128 v195, v[152:155] offset:0\n\t"
    "s_waitcnt lgkmcnt(8)\n\t"
    "v_mfma_f32_16x16x32_bf16 v[64:67], a[140:143], a[208:211], v[64:67]\n\t"
    "v_add_f32 v144, v112, v104\n\t"
    "v_add_f32 v145, v113, v105\n\t"
    "v_mfma_f32_16x16x32_bf16 v[64:67], a[144:147], a[212:215], v[64:67]\n\t"
    "v_add_f32 v146, v114, v106\n\t"
    "v_add_f32 v147, v115, v107\n\t"
    "v_mfma_f32_16x16x32_bf16 v[64:67], a[148:151], a[216:219], v[64:67]\n\t"
    "ds_read_b128 v[40:43], v197 offset:1024\n\t"
    "v_cvt_pk_bf16_f32 v156, v144, v145\n\t"
    "v_mfma_f32_16x16x32_bf16 v[0:3], a[60:63], a[192:195], v[0:3]\n\t"
    "ds_read_b128 v[44:47], v197 offset:2048\n\t"
    "s_add_u32 s76, s74, 0\n\t"
    "v_cvt_pk_bf16_f32 v157, v146, v147\n\t"
    "v_mfma_f32_16x16x32_bf16 v[4:7], a[40:43], a[184:187], v[4:7]\n\t"
    "v_add_f32 v148, v116, v108\n\t"
    "v_add_f32 v149, v117, v109\n\t"
    "v_mfma_f32_16x16x32_bf16 v[4:7], a[52:55], a[188:191], v[4:7]\n\t"
    "v_add_f32 v150, v118, v110\n\t"
    "v_add_f32 v151, v119, v111\n\t"
    "v_mfma_f32_16x16x32_bf16 v[4:7], a[64:67], a[192:195], v[4:7]\n\t"
    "v_cvt_pk_bf16_f32 v158, v148, v149\n\t"
    "v_cvt_pk_bf16_f32 v159, v150, v151\n\t"
    "v_mfma_f32_16x16x32_bf16 v[8:11], a[44:47], a[184:187], v[8:11]\n\t"
    "ds_write_b128 v195, v[156:159] offset:9600\n\t"
    "v_mfma_f32_16x16x32_bf16 v[8:11], a[56:59], a[188:191], v[8:11]\n\t"
    "v_sub_f32 v144, v112, v104\n\t"
    "v_sub_f32 v145, v113, v105\n\t"
    "v_mfma_f32_16x16x32_bf16 v[8:11], a[68:71], a[192:195], v[8:11]\n\t"
    "v_sub_f32 v146, v114, v106\n\t"
    "v_sub_f32 v147, v115, v107\n\t"
    "v_mfma_f32_16x16x32_bf16 v[0:3], a[72:75], a[196:199], v[0:3]\n\t"
    "ds_read_b128 a[184:187], v192 offset:3072\n\t"
    "v_cvt_pk_bf16_f32 v160, v144, v145\n\t"
    "v_mfma_f32_16x16x32_bf16 v[0:3], a[84:87], a[200:203], v[0:3]\n\t"
    "ds_read_b128 a[188:191], v192 offset:3136\n\t"
    "v_cvt_pk_bf16_f32 v161, v146, v147\n\t"
    "v_mfma_f32_16x16x32_bf16 v[0:3], a[96:99], a[204:207], v[0:3]\n\t"
    "ds_read_b128 a[192:195], v192 offset:3200\n\t"
    "v_sub_f32 v148, v116, v108\n\t"
    "v_mfma_f32_16x16x32_bf16 v[4:7], a[76:79], a[196:199], v[4:7]\n\t"
    "v_sub_f32 v149, v117, v109\n\t"
    "v_sub_f32 v150, v118, v110\n\t"
    "v_mfma_f32_16x16x32_bf16 v[4:7], a[88:91], a[200:203], v[4:7]\n\t"
    "v_sub_f32 v151, v119, v111\n\t"
    "v_cvt_pk_bf16_f32 v162, v148, v149\n\t"
    "v_mfma_f32_16x16x32_bf16 v[4:7], a[100:103], a[204:207], v[4:7]\n\t"
    "s_waitcnt lgkmcnt(4)\n\t"
    "v_fma_f32 v36, v36, s75, v0\n\t"
    "v_fma_f32 v37, v37, s75, v1\n\t"
    "v_mfma_f32_16x16x32_bf16 v[8:11], a[80:83], a[196:199], v[8:11]\n\t"
    "ds_read_b128 a[196:199], v193 offset:3072\n\t"
    "v_fma_f32 v38, v38, s75, v2\n\t"
    "v_mfma_f32_16x16x32_bf16 v[8:11], a[92:95], a[200:203], v[8:11]\n\t"
    "ds_read_b128 a[200:203], v193 offset:3136\n\t"
    "v_fma_f32 v39, v39, s75, v3\n\t"
    "v_mfma_f32_16x16x32_bf16 v[8:11], a[104:107], a[204:207], v[8:11]\n\t"
    "ds_read_b128 a[204:207], v193 offset:3200\n\t"
    "v_cvt_pk_bf16_f32 v36, v36, v37\n\t"
    "v_mfma_f32_16x16x32_bf16 v[12:15], a[0:3], a[172:175], v[12:15]\n\t"
    "v_cvt_pk_bf16_f32 v37, v38, v39\n\t"
    "v_fma_f32 v40, v40, s75, v4\n\t"
    "v_mfma_f32_16x16x32_bf16 v[12:15], a[12:15], a[176:179], v[12:15]\n\t"
    "v_fma_f32 v41, v41, s75, v5\n\t"
    "v_fma_f32 v42, v42, s75, v6\n\t"
    "v_mfma_f32_16x16x32_bf16 v[12:15], a[24:27], a[180:183], v[12:15]\n\t"
    "v_fma_f32 v43, v43, s75, v7\n\t"
    "v_cvt_pk_bf16_f32 v38, v40, v41\n\t"
    "v_mfma_f32_16x16x32_bf16 v[16:19], a[4:7], a[172:175], v[16:19]\n\t"
    "v_cvt_pk_bf16_f32 v39, v42, v43\n\t"
    "v_cvt_pk_bf16_f32 v163, v150, v151\n\t"
    "v_mfma_f32_16x16x32_bf16 v[16:19], a[16:19], a[176:179], v[16:19]\n\t"
    "buffer_store_dwordx4 v[36:39], v200, s[52:55], s76 offen offset:0 sc1\n\t"
    "v_mfma_f32_16x16x32_bf16 v[16:19], a[28:31], a[180:183], v[16:19]\n\t"
    "ds_write_b128 v195, v[160:163] offset:19200\n\t"
    "v_mfma_f32_16x16x32_bf16 v[20:23], a[8:11], a[172:175], v[20:23]\n\t"
    "v_fma_f32 v44, v44, s75, v8\n\t"
    "v_fma_f32 v45, v45, s75, v9\n\t"
    "v_mfma_f32_16x16x32_bf16 v[20:23], a[20:23], a[176:179], v[20:23]\n\t"
    "v_fma_f32 v46, v46, s75, v10\n\t"
    "v_fma_f32 v47, v47, s75, v11\n\t"
    "v_mfma_f32_16x16x32_bf16 v[20:23], a[32:35], a[180:183], v[20:23]\n\t"
    "ds_read_b128 a[172:175], v191 offset:6144\n\t"
    "v_cvt_pk_bf16_f32 v44, v44, v45\n\t"
    "s_waitcnt lgkmcnt(5)\n\t"
    "v_mfma_f32_16x16x32_bf16 v[12:15], a[36:39], a[184:187], v[12:15]\n\t"
    "ds_read_b128 a[176:179], v191 offset:6208\n\t"
    "v_cvt_pk_bf16_f32 v45, v46, v47\n\t"
    "v_mfma_f32_16x16x32_bf16 v[12:15], a[48:51], a[188:191], v[12:15]\n\t"
    "ds_write_b64 v210, v[44:45] offset:0\n\t"
    "v_mfma_f32_16x16x32_bf16 v[12:15], a[60:63], a[192:195], v[12:15]\n\t"
    "v_sub_f32 v144, v88, v112\n\t"
    "v_sub_f32 v145, v89, v113\n\t"
    "v_mfma_f32_16x16x32_bf16 v[16:19], a[40:43], a[184:187], v[16:19]\n\t"
    "ds_read_b128 a[180:183], v191 offset:6272\n\t"
    "v_sub_f32 v146, v90, v114\n\t"
    "v_mfma_f32_16x16x32_bf16 v[16:19], a[52:55], a[188:191], v[16:19]\n\t"
    "ds_read_b128 v[36:39], v197 offset:12288\n\t"
    "v_sub_f32 v147, v91, v115\n\t"
    "v_mfma_f32_16x16x32_bf16 v[112:115], a[164:167], a[120:123], 0\n\t"
    "ds_read_b128 v[40:43], v197 offset:13312\n\t"
    "v_cvt_pk_bf16_f32 v152, v144, v145\n\t"
    "v_mfma_f32_16x16x32_bf16 v[112:115], a[140:143], a[220:223], v[112:115]\n\t"
    "ds_read_b128 v[44:47], v197 offset:14336\n\t"
    "s_add_u32 s77, s74, 0xc00\n\t"
    "v_cvt_pk_bf16_f32 v153, v146, v147\n\t"
    "v_mfma_f32_16x16x32_bf16 v[112:115], a[144:147], a[224:227], v[112:115]\n\t"
    "ds_read_b128 a[140:143], v202 offset:12288\n\t"
    "v_sub_f32 v148, v92, v116\n\t"
    "v_mfma_f32_16x16x32_bf16 v[112:115], a[148:151], a[228:231], v[112:115]\n\t"
    "ds_read_b128 a[144:147], v202 offset:13312\n\t"
    "v_sub_f32 v149, v93, v117\n\t"
    "v_mfma_f32_16x16x32_bf16 v[68:71], a[166:169], a[132:135], 0\n\t"
    "buffer_load_dwordx4 a[132:135], v199, s[40:43], s81 offen offset:64\n\t"
    "s_add_u32 s78, s73, 0x1b000\n\t"
    "v_mfma_f32_16x16x32_bf16 v[68:71], a[152:155], a[208:211], v[68:71]\n\t"
    "ds_read_b128 a[148:151], v202 offset:14336\n\t"
    "v_sub_f32 v150, v94, v118\n\t"
    "v_mfma_f32_16x16x32_bf16 v[68:71], a[156:159], a[212:215], v[68:71]\n\t"
    "v_sub_f32 v151, v95, v119\n\t"
    "v_cvt_pk_bf16_f32 v154, v148, v149\n\t"
    "v_mfma_f32_16x16x32_bf16 v[68:71], a[160:163], a[216:219], v[68:71]\n\t"
    "v_cvt_pk_bf16_f32 v155, v150, v151\n\t"
    "v_mfma_f32_16x16x32_bf16 v[116:119], a[166:169], a[120:123], 0\n\t"
    "ds_write_b128 v195, v[152:155] offset:28800\n\t"
    "v_mfma_f32_16x16x32_bf16 v[116:119], a[152:155], a[220:223], v[116:119]\n\t"
    "v_sub_f32 v144, v80, v64\n\t"
    "v_sub_f32 v145, v81, v65\n\t"
    "v_mfma_f32_16x16x32_bf16 v[116:119], a[156:159], a[224:227], v[116:119]\n\t"
    "v_sub_f32 v146, v82, v66\n\t"
    "v_sub_f32 v147, v83, v67\n\t"
    "v_mfma_f32_16x16x32_bf16 v[116:119], a[160:163], a[228:231], v[116:119]\n\t"
    "ds_read_b128 a[152:155], v202 offset:15360\n\t"
    "v_cvt_pk_bf16_f32 v156, v144, v145\n\t"
    "v_mfma_f32_16x16x32_bf16 v[80:83], a[164:167], a[136:139], 0\n\t"
    "ds_read_b128 a[156:159], v202 offset:16384\n\t"
    "v_cvt_pk_bf16_f32 v157, v146, v147\n\t"
    "v_mfma_f32_16x16x32_bf16 v[16:19], a[64:67], a[192:195], v[16:19]\n\t"
    "ds_read_b128 a[160:163], v202 offset:17408\n\t"
    "v_sub_f32 v148, v84, v68\n\t"
    "v_mfma_f32_16x16x32_bf16 v[20:23], a[44:47], a[184:187], v[20:23]\n\t"
    "ds_read_b128 a[184:187], v192 offset:6144\n\t"
    "v_sub_f32 v149, v85, v69\n\t"
    "v_mfma_f32_16x16x32_bf16 v[20:23], a[56:59], a[188:191], v[20:23]\n\t"
    "ds_read_b128 a[188:191], v192 offset:6208\n\t"
    "v_sub_f32 v150, v86, v70\n\t"
    "s_waitcnt lgkmcnt(6)\n\t"
    "v_mfma_f32_16x16x32_bf16 v[80:83], a[140:143], a[208:211], v[80:83]\n\t"
    "v_sub_f32 v151, v87, v71\n\t"
    "v_cvt_pk_bf16_f32 v158, v148, v149\n\t"
    "v_mfma_f32_16x16x32_bf16 v[80:83], a[144:147], a[212:215], v[80:83]\n\t"
    "v_cvt_pk_bf16_f32 v159, v150, v151\n\t"
    "v_mfma_f32_16x16x32_bf16 v[80:83], a[148:151], a[216:219], v[80:83]\n\t"
    "ds_write_b128 v195, v[156:159] offset:64\n\t"
    "v_mfma_f32_16x16x32_bf16 v[20:23], a[68:71], a[192:195], v[20:23]\n\t"
    "v_add_f32 v144, v56, v64\n\t"
    "v_add_f32 v145, v57, v65\n\t"
    "v_mfma_f32_16x16x32_bf16 v[12:15], a[72:75], a[196:199], v[12:15]\n\t"
    "v_add_f32 v146, v58, v66\n\t"
    "v_add_f32 v147, v59, v67\n\t"
    "v_mfma_f32_16x16x32_bf16 v[12:15], a[84:87], a[200:203], v[12:15]\n\t"
    "ds_read_b128 a[192:195], v192 offset:6272\n\t"
    "v_cvt_pk_bf16_f32 v160, v144, v145\n\t"
    "v_mfma_f32_16x16x32_bf16 v[12:15], a[96:99], a[204:207], v[12:15]\n\t"
    "v_cvt_pk_bf16_f32 v161, v146, v147\n\t"
    "v_add_f32 v148, v60, v68\n\t"
    "v_mfma_f32_16x16x32_bf16 v[16:19], a[76:79], a[196:199], v[16:19]\n\t"
    "v_add_f32 v149, v61, v69\n\t"
    "v_add_f32 v150, v62, v70\n\t"
    "v_mfma_f32_16x16x32_bf16 v[16:19], a[88:91], a[200:203], v[16:19]\n\t"
    "v_add_f32 v151, v63, v71\n\t"
    "v_cvt_pk_bf16_f32 v162, v148, v149\n\t"
    "v_mfma_f32_16x16x32_bf16 v[16:19], a[100:103], a[204:207], v[16:19]\n\t"
    "v_fma_f32 v36, v36, s75, v12\n\t"
    "v_fma_f32 v37, v37, s75, v13\n\t"
    "v_mfma_f32_16x16x32_bf16 v[20:23], a[80:83], a[196:199], v[20:23]\n\t"
    "ds_read_b128 a[196:199], v193 offset:6144\n\t"
    "v_fma_f32 v38, v38, s75, v14\n\t"
    "v_mfma_f32_16x16x32_bf16 v[20:23], a[92:95], a[200:203], v[20:23]\n\t"
    "ds_read_b128 a[200:203], v193 offset:6208\n\t"
    "v_fma_f32 v39, v39, s75, v15\n\t"
    "v_mfma_f32_16x16x32_bf16 v[20:23], a[104:107], a[204:207], v[20:23]\n\t"
    "ds_read_b128 a[204:207], v193 offset:6272\n\t"
    "v_cvt_pk_bf16_f32 v36, v36, v37\n\t"
    "v_mfma_f32_16x16x32_bf16 v[24:27], a[0:3], a[172:175], v[24:27]\n\t"
    "v_cvt_pk_bf16_f32 v37, v38, v39\n\t"
    "v_fma_f32 v40, v40, s75, v16\n\t"
    "v_mfma_f32_16x16x32_bf16 v[24:27], a[12:15], a[176:179], v[24:27]\n\t"
    "v_fma_f32 v41, v41, s75, v17\n\t"
    "v_fma_f32 v42, v42, s75, v18\n\t"
    "v_mfma_f32_16x16x32_bf16 v[24:27], a[24:27], a[180:183], v[24:27]\n\t"
    "v_fma_f32 v43, v43, s75, v19\n\t"
    "v_cvt_pk_bf16_f32 v38, v40, v41\n\t"
    "v_mfma_f32_16x16x32_bf16 v[28:31], a[4:7], a[172:175], v[28:31]\n\t"
    "v_cvt_pk_bf16_f32 v39, v42, v43\n\t"
    "v_cvt_pk_bf16_f32 v163, v150, v151\n\t"
    "v_mfma_f32_16x16x32_bf16 v[28:31], a[16:19], a[176:179], v[28:31]\n\t"
    "buffer_store_dwordx4 v[36:39], v200, s[52:55], s77 offen offset:0 sc1\n\t"
    "v_mfma_f32_16x16x32_bf16 v[28:31], a[28:31], a[180:183], v[28:31]\n\t"
    "ds_write_b128 v195, v[160:163] offset:9664\n\t"
    "v_mfma_f32_16x16x32_bf16 v[32:35], a[8:11], a[172:175], v[32:35]\n\t"
    "v_fma_f32 v44, v44, s75, v20\n\t"
    "v_fma_f32 v45, v45, s75, v21\n\t"
    "v_mfma_f32_16x16x32_bf16 v[32:35], a[20:23], a[176:179], v[32:35]\n\t"
    "v_fma_f32 v46, v46, s75, v22\n\t"
    "v_fma_f32 v47, v47, s75, v23\n\t"
    "v_mfma_f32_16x16x32_bf16 v[32:35], a[32:35], a[180:183], v[32:35]\n\t"
    "v_cvt_pk_bf16_f32 v44, v44, v45\n\t"
    "v_cvt_pk_bf16_f32 v45, v46, v47\n\t"
    "ds_write_b64 v210, v[44:45] offset:2048\n\t"
    "v_sub_f32 v144, v56, v64\n\t"
    "v_sub_f32 v145, v57, v65\n\t"
    "v_sub_f32 v146, v58, v66\n\t"
    "v_sub_f32 v147, v59, v67\n\t"
    "v_cvt_pk_bf16_f32 v152, v144, v145\n\t"
    "ds_read_b128 v[36:39], v197 offset:24576\n\t"
    "ds_read_b128 v[40:43], v197 offset:25600\n\t"
    "ds_read_b128 v[44:47], v197 offset:26624\n\t"
    "s_add_u32 s76, s74, 0x1800\n\t"
    "v_cvt_pk_bf16_f32 v153, v146, v147\n\t"
    "v_sub_f32 v148, v60, v68\n\t"
    "v_sub_f32 v149, v61, v69\n\t"
    "v_sub_f32 v150, v62, v70\n\t"
    "v_sub_f32 v151, v63, v71\n\t"
    "v_cvt_pk_bf16_f32 v154, v148, v149\n\t"
    "v_cvt_pk_bf16_f32 v155, v150, v151\n\t"
    "ds_write_b128 v195, v[152:155] offset:19264\n\t"
    "v_sub_f32 v144, v112, v56\n\t"
    "v_sub_f32 v145, v113, v57\n\t"
    "v_sub_f32 v146, v114, v58\n\t"
    "v_sub_f32 v147, v115, v59\n\t"
    "v_mfma_f32_16x16x32_bf16 v[56:59], a[164:167], a[124:127], 0\n\t"
    "v_cvt_pk_bf16_f32 v156, v144, v145\n\t"
    "v_cvt_pk_bf16_f32 v157, v146, v147\n\t"
    "v_mfma_f32_16x16x32_bf16 v[56:59], a[140:143], a[220:223], v[56:59]\n\t"
    "ds_read_b128 a[140:143], v202 offset:0\n\t"
    "v_sub_f32 v148, v116, v60\n\t"
    "v_mfma_f32_16x16x32_bf16 v[56:59], a[144:147], a[224:227], v[56:59]\n\t"
    "ds_read_b128 a[144:147], v202 offset:1024\n\t"
    "v_sub_f32 v149, v117, v61\n\t"
    "v_mfma_f32_16x16x32_bf16 v[56:59], a[148:151], a[228:231], v[56:59]\n\t"
    "ds_read_b128 a[148:151], v202 offset:2048\n\t"
    "v_sub_f32 v150, v118, v62\n\t"
    "v_mfma_f32_16x16x32_bf16 v[84:87], a[166:169], a[136:139], 0\n\t"
    "buffer_load_dwordx4 a[136:139], v199, s[40:43], s78 offen offset:128\n\t"
    "s_add_u32 s79, s70, 0x7e000\n\t"
    "s_waitcnt lgkmcnt(15)\n\t"
    "v_mfma_f32_16x16x32_bf16 v[84:87], a[152:155], a[208:211], v[84:87]\n\t"
    "buffer_load_dwordx4 a[208:211], v198, s[36:39], s79 offen offset:0\n\t"
    "v_mfma_f32_16x16x32_bf16 v[84:87], a[156:159], a[212:215], v[84:87]\n\t"
    "buffer_load_dwordx4 a[212:215], v198, s[36:39], s79 offen offset:64\n\t"
    "v_mfma_f32_16x16x32_bf16 v[84:87], a[160:163], a[216:219], v[84:87]\n\t"
    "buffer_load_dwordx4 a[216:219], v198, s[36:39], s79 offen offset:128\n\t"
    "s_add_u32 s80, s70, 0x90000\n\t"
    "v_sub_f32 v151, v119, v63\n\t"
    "v_mfma_f32_16x16x32_bf16 v[60:63], a[166:169], a[124:127], 0\n\t"
    "v_cvt_pk_bf16_f32 v158, v148, v149\n\t"
    "v_cvt_pk_bf16_f32 v159, v150, v151\n\t"
    "v_mfma_f32_16x16x32_bf16 v[60:63], a[152:155], a[220:223], v[60:63]\n\t"
    "ds_write_b128 v195, v[156:159] offset:28864\n\t"
    "v_mfma_f32_16x16x32_bf16 v[60:63], a[156:159], a[224:227], v[60:63]\n\t"
    "buffer_load_dwordx4 a[220:223], v198, s[36:39], s80 offen offset:0\n\t"
    "v_mfma_f32_16x16x32_bf16 v[60:63], a[160:163], a[228:231], v[60:63]\n\t"
    "buffer_load_dwordx4 a[224:227], v198, s[36:39], s80 offen offset:64\n\t"
    "buffer_load_dwordx4 a[228:231], v198, s[36:39], s80 offen offset:128\n\t"
    "s_add_u32 s81, s73, 0x24000\n\t"
    "ds_read_b128 a[152:155], v202 offset:3072\n\t"
    "ds_read_b128 a[156:159], v202 offset:4096\n\t"
    "ds_read_b128 a[160:163], v202 offset:5120\n\t"
    "v_sub_f32 v144, v96, v80\n\t"
    "v_sub_f32 v145, v97, v81\n\t"
    "v_sub_f32 v146, v98, v82\n\t"
    "v_sub_f32 v147, v99, v83\n\t"
    "v_mfma_f32_16x16x32_bf16 v[96:99], a[164:167], a[116:119], 0\n\t"
    "v_cvt_pk_bf16_f32 v160, v144, v145\n\t"
    "v_cvt_pk_bf16_f32 v161, v146, v147\n\t"
    "s_waitcnt vmcnt(14) lgkmcnt(4)\n\t"
    "v_mfma_f32_16x16x32_bf16 v[96:99], a[140:143], a[232:235], v[96:99]\n\t"
    "v_sub_f32 v148, v100, v84\n\t"
    "v_sub_f32 v149, v101, v85\n\t"
    "v_mfma_f32_16x16x32_bf16 v[96:99], a[144:147], a[236:239], v[96:99]\n\t"
    "v_sub_f32 v150, v102, v86\n\t"
    "v_sub_f32 v151, v103, v87\n\t"
    "v_mfma_f32_16x16x32_bf16 v[96:99], a[148:151], a[240:243], v[96:99]\n\t"
    "v_cvt_pk_bf16_f32 v162, v148, v149\n\t"
    "v_cvt_pk_bf16_f32 v163, v150, v151\n\t"
    "ds_write_b128 v195, v[160:163] offset:128\n\t"
    "v_add_f32 v144, v72, v80\n\t"
    "v_add_f32 v145, v73, v81\n\t"
    "v_add_f32 v146, v74, v82\n\t"
    "v_add_f32 v147, v75, v83\n\t"
    "v_cvt_pk_bf16_f32 v152, v144, v145\n\t"
    "v_cvt_pk_bf16_f32 v153, v146, v147\n\t"
    "v_add_f32 v148, v76, v84\n\t"
    "v_add_f32 v149, v77, v85\n\t"
    "v_add_f32 v150, v78, v86\n\t"
    "v_add_f32 v151, v79, v87\n\t"
    "v_cvt_pk_bf16_f32 v154, v148, v149\n\t"
    "v_cvt_pk_bf16_f32 v155, v150, v151\n\t"
    "ds_write_b128 v195, v[152:155] offset:9728\n\t"
    "v_sub_f32 v144, v72, v80\n\t"
    "v_sub_f32 v145, v73, v81\n\t"
    "v_sub_f32 v146, v74, v82\n\t"
    "v_sub_f32 v147, v75, v83\n\t"
    "v_cvt_pk_bf16_f32 v156, v144, v145\n\t"
    "v_cvt_pk_bf16_f32 v157, v146, v147\n\t"
    "v_sub_f32 v148, v76, v84\n\t"
    "v_sub_f32 v149, v77, v85\n\t"
    "v_sub_f32 v150, v78, v86\n\t"
    "v_sub_f32 v151, v79, v87\n\t"
    "v_cvt_pk_bf16_f32 v158, v148, v149\n\t"
    "v_cvt_pk_bf16_f32 v159, v150, v151\n\t"
    "ds_write_b128 v195, v[156:159] offset:19328\n\t"
    "v_sub_f32 v144, v56, v72\n\t"
    "v_sub_f32 v145, v57, v73\n\t"
    "v_sub_f32 v146, v58, v74\n\t"
    "v_sub_f32 v147, v59, v75\n\t"
    "s_waitcnt vmcnt(10)\n\t"
    "v_mfma_f32_16x16x32_bf16 v[72:75], a[164:167], a[128:131], 0\n\t"
    "v_cvt_pk_bf16_f32 v160, v144, v145\n\t"
    "v_cvt_pk_bf16_f32 v161, v146, v147\n\t"
    "v_mfma_f32_16x16x32_bf16 v[72:75], a[140:143], a[244:247], v[72:75]\n\t"
    "ds_read_b128 a[140:143], v202 offset:6144\n\t"
    "v_sub_f32 v148, v60, v76\n\t"
    "v_mfma_f32_16x16x32_bf16 v[72:75], a[144:147], a[248:251], v[72:75]\n\t"
    "ds_read_b128 a[144:147], v202 offset:7168\n\t"
    "v_sub_f32 v149, v61, v77\n\t"
    "v_mfma_f32_16x16x32_bf16 v[72:75], a[148:151], a[252:255], v[72:75]\n\t"
    "ds_read_b128 a[148:151], v202 offset:8192\n\t"
    "v_sub_f32 v150, v62, v78\n\t"
    "v_mfma_f32_16x16x32_bf16 v[100:103], a[166:169], a[116:119], 0\n\t"
    "v_sub_f32 v151, v63, v79\n\t"
    "v_cvt_pk_bf16_f32 v162, v148, v149\n\t"
    "s_waitcnt lgkmcnt(6)\n\t"
    "v_mfma_f32_16x16x32_bf16 v[100:103], a[152:155], a[232:235], v[100:103]\n\t"
    "v_cvt_pk_bf16_f32 v163, v150, v151\n\t"
    "v_mfma_f32_16x16x32_bf16 v[100:103], a[156:159], a[236:239], v[100:103]\n\t"
    "ds_write_b128 v195, v[160:163] offset:28928\n\t"
    "v_mfma_f32_16x16x32_bf16 v[100:103], a[160:163], a[240:243], v[100:103]\n\t"
    "v_sub_f32 v144, v104, v96\n\t"
    "v_sub_f32 v145, v105, v97\n\t"
    "v_mfma_f32_16x16x32_bf16 v[76:79], a[166:169], a[128:131], 0\n\t"
    "v_sub_f32 v146, v106, v98\n\t"
    "v_sub_f32 v147, v107, v99\n\t"
    "v_mfma_f32_16x16x32_bf16 v[76:79], a[152:155], a[244:247], v[76:79]\n\t"
    "ds_read_b128 a[152:155], v202 offset:9216\n\t"
    "v_cvt_pk_bf16_f32 v152, v144, v145\n\t"
    "v_mfma_f32_16x16x32_bf16 v[76:79], a[156:159], a[248:251], v[76:79]\n\t"
    "ds_read_b128 a[156:159], v202 offset:10240\n\t"
    "v_cvt_pk_bf16_f32 v153, v146, v147\n\t"
    "v_mfma_f32_16x16x32_bf16 v[76:79], a[160:163], a[252:255], v[76:79]\n\t"
    "ds_read_b128 a[160:163], v202 offset:11264\n\t"
    "v_sub_f32 v148, v108, v100\n\t"
    "v_mfma_f32_16x16x32_bf16 v[104:107], a[164:167], a[120:123], 0\n\t"
    "buffer_load_dwordx4 a[116:119], v199, s[40:43], s81 offen offset:0\n\t"
    "s_add_u32 s78, s73, 0x24000\n\t"
    "v_sub_f32 v149, v109, v101\n\t"
    "v_sub_f32 v150, v110, v102\n\t"
    "v_sub_f32 v151, v111, v103\n\t"
    "v_cvt_pk_bf16_f32 v154, v148, v149\n\t"
    "s_waitcnt lgkmcnt(4)\n\t"
    "v_mfma_f32_16x16x32_bf16 v[104:107], a[140:143], a[232:235], v[104:107]\n\t"
    "v_cvt_pk_bf16_f32 v155, v150, v151\n\t"
    "v_mfma_f32_16x16x32_bf16 v[104:107], a[144:147], a[236:239], v[104:107]\n\t"
    "v_mfma_f32_16x16x32_bf16 v[104:107], a[148:151], a[240:243], v[104:107]\n\t"
    "s_waitcnt lgkmcnt(3)\n\t"
    "s_barrier\n\t"
    "v_mfma_f32_16x16x32_bf16 v[24:27], a[36:39], a[184:187], v[24:27]\n\t"
    "ds_write_b128 v194, v[152:155] offset:0\n\t"
    "v_mfma_f32_16x16x32_bf16 v[24:27], a[48:51], a[188:191], v[24:27]\n\t"
    "v_add_f32 v144, v88, v96\n\t"
    "v_add_f32 v145, v89, v97\n\t"
    "v_mfma_f32_16x16x32_bf16 v[24:27], a[60:63], a[192:195], v[24:27]\n\t"
    "v_add_f32 v146, v90, v98\n\t"
    "v_add_f32 v147, v91, v99\n\t"
    "v_mfma_f32_16x16x32_bf16 v[28:31], a[40:43], a[184:187], v[28:31]\n\t"
    "ds_read_b128 a[172:175], v188 offset:38400\n\t"
    "v_cvt_pk_bf16_f32 v156, v144, v145\n\t"
    "v_mfma_f32_16x16x32_bf16 v[28:31], a[52:55], a[188:191], v[28:31]\n\t"
    "ds_read_b128 a[176:179], v188 offset:38464\n\t"
    "v_cvt_pk_bf16_f32 v157, v146, v147\n\t"
    "v_mfma_f32_16x16x32_bf16 v[28:31], a[64:67], a[192:195], v[28:31]\n\t"
    "ds_read_b128 a[180:183], v188 offset:38528\n\t"
    "v_add_f32 v148, v92, v100\n\t"
    "v_mfma_f32_16x16x32_bf16 v[32:35], a[44:47], a[184:187], v[32:35]\n\t"
    "ds_read_b128 a[184:187], v189 offset:38400\n\t"
    "v_add_f32 v149, v93, v101\n\t"
    "v_mfma_f32_16x16x32_bf16 v[32:35], a[56:59], a[188:191], v[32:35]\n\t"
    "ds_read_b128 a[188:191], v189 offset:38464\n\t"
    "v_add_f32 v150, v94, v102\n\t"
    "v_mfma_f32_16x16x32_bf16 v[32:35], a[68:71], a[192:195], v[32:35]\n\t"
    "ds_read_b128 a[192:195], v189 offset:38528\n\t"
    "v_add_f32 v151, v95, v103\n\t"
    "v_mfma_f32_16x16x32_bf16 v[24:27], a[72:75], a[196:199], v[24:27]\n\t"
    "ds_read_b64 a[170:171], v211 offset:0\n\t"
    "s_add_u32 s86, s74, 0\n\t"
    "v_cvt_pk_bf16_f32 v158, v148, v149\n\t"
    "v_mfma_f32_16x16x32_bf16 v[24:27], a[84:87], a[200:203], v[24:27]\n\t"
    "v_cvt_pk_bf16_f32 v159, v150, v151\n\t"
    "v_mfma_f32_16x16x32_bf16 v[24:27], a[96:99], a[204:207], v[24:27]\n\t"
    "ds_write_b128 v194, v[156:159] offset:9600\n\t"
    "v_mfma_f32_16x16x32_bf16 v[28:31], a[76:79], a[196:199], v[28:31]\n\t"
    "v_sub_f32 v144, v88, v96\n\t"
    "v_sub_f32 v145, v89, v97\n\t"
    "v_mfma_f32_16x16x32_bf16 v[28:31], a[88:91], a[200:203], v[28:31]\n\t"
    "v_sub_f32 v146, v90, v98\n\t"
    "v_sub_f32 v147, v91, v99\n\t"
    "v_mfma_f32_16x16x32_bf16 v[28:31], a[100:103], a[204:207], v[28:31]\n\t"
    "v_fma_f32 v36, v36, s75, v24\n\t"
    "v_fma_f32 v37, v37, s75, v25\n\t"
    "v_mfma_f32_16x16x32_bf16 v[32:35], a[80:83], a[196:199], v[32:35]\n\t"
    "ds_read_b128 a[196:199], v190 offset:38400\n\t"
    "v_fma_f32 v38, v38, s75, v26\n\t"
    "v_mfma_f32_16x16x32_bf16 v[32:35], a[92:95], a[200:203], v[32:35]\n\t"
    "ds_read_b128 a[200:203], v190 offset:38464\n\t"
    "v_fma_f32 v39, v39, s75, v27\n\t"
    "v_mfma_f32_16x16x32_bf16 v[32:35], a[104:107], a[204:207], v[32:35]\n\t"
    "ds_read_b128 a[204:207], v190 offset:38528\n\t"
    "v_cvt_pk_bf16_f32 v36, v36, v37\n\t"
    "s_waitcnt lgkmcnt(8)\n\t"
    "v_mfma_f32_16x16x32_bf16 v[0:3], a[108:111], a[172:175], 0\n\t"
    "v_cvt_pk_bf16_f32 v37, v38, v39\n\t"
    "v_fma_f32 v40, v40, s75, v28\n\t"
    "v_mfma_f32_16x16x32_bf16 v[0:3], v[216:219], a[176:179], v[0:3]\n\t"
    "v_fma_f32 v41, v41, s75, v29\n\t"
    "v_fma_f32 v42, v42, s75, v30\n\t"
    "v_mfma_f32_16x16x32_bf16 v[0:3], v[228:231], a[180:183], v[0:3]\n\t"
    "s_waitcnt lgkmcnt(4)\n\t"
    "buffer_store_dwordx2 a[170:171], v201, s[52:55], s86 offen offset:0 sc1\n\t"
    "v_mfma_f32_16x16x32_bf16 v[4:7], a[112:115], a[172:175], 0\n\t"
    "v_fma_f32 v43, v43, s75, v31\n\t"
    "v_cvt_pk_bf16_f32 v38, v40, v41\n\t"
    "v_mfma_f32_16x16x32_bf16 v[4:7], v[220:223], a[176:179], v[4:7]\n\t"
    "v_cvt_pk_bf16_f32 v39, v42, v43\n\t"
    "v_cvt_pk_bf16_f32 v160, v144, v145\n\t"
    "v_mfma_f32_16x16x32_bf16 v[4:7], v[232:235], a[180:183], v[4:7]\n\t"
    "v_cvt_pk_bf16_f32 v161, v146, v147\n\t"
    "v_sub_f32 v148, v92, v100\n\t"
    "v_mfma_f32_16x16x32_bf16 v[8:11], v[212:215], a[172:175], 0\n\t"
    "ds_read_b128 a[172:175], v188 offset:41472\n\t"
    "v_sub_f32 v149, v93, v101\n\t"
    "v_mfma_f32_16x16x32_bf16 v[8:11], v[224:227], a[176:179], v[8:11]\n\t"
    "ds_read_b128 a[176:179], v188 offset:41536\n\t"
    "v_sub_f32 v150, v94, v102\n\t"
    "v_mfma_f32_16x16x32_bf16 v[8:11], v[236:239], a[180:183], v[8:11]\n\t"
    "ds_read_b128 a[180:183], v188 offset:41600\n\t"
    "v_sub_f32 v151, v95, v103\n\t"
    "v_mfma_f32_16x16x32_bf16 v[0:3], v[240:243], a[184:187], v[0:3]\n\t"
    "v_cvt_pk_bf16_f32 v162, v148, v149\n\t"
    "v_cvt_pk_bf16_f32 v163, v150, v151\n\t"
    "v_mfma_f32_16x16x32_bf16 v[0:3], v[252:255], a[188:191], v[0:3]\n\t"
    "ds_write_b128 v194, v[160:163] offset:19200\n\t"
    "v_mfma_f32_16x16x32_bf16 v[0:3], v[128:131], a[192:195], v[0:3]\n\t"
    "v_sub_f32 v144, v72, v88\n\t"
    "v_sub_f32 v145, v73, v89\n\t"
    "v_mfma_f32_16x16x32_bf16 v[4:7], v[244:247], a[184:187], v[4:7]\n\t"
    "v_sub_f32 v146, v74, v90\n\t"
    "v_sub_f32 v147, v75, v91\n\t"
    "s_waitcnt vmcnt(10)\n\t"
    "v_mfma_f32_16x16x32_bf16 v[88:91], a[164:167], a[132:135], 0\n\t"
    "v_cvt_pk_bf16_f32 v152, v144, v145\n\t"
    "v_cvt_pk_bf16_f32 v153, v146, v147\n\t"
    "v_mfma_f32_16x16x32_bf16 v[88:91], a[140:143], a[244:247], v[88:91]\n\t"
    "ds_read_b128 a[140:143], v202 offset:12288\n\t"
    "v_sub_f32 v148, v76, v92\n\t"
    "v_mfma_f32_16x16x32_bf16 v[88:91], a[144:147], a[248:251], v[88:91]\n\t"
    "ds_read_b128 a[144:147], v202 offset:13312\n\t"
    "v_sub_f32 v149, v77, v93\n\t"
    "v_mfma_f32_16x16x32_bf16 v[88:91], a[148:151], a[252:255], v[88:91]\n\t"
    "ds_read_b128 a[148:151], v202 offset:14336\n\t"
    "v_sub_f32 v150, v78, v94\n\t"
    "v_mfma_f32_16x16x32_bf16 v[108:111], a[166:169], a[120:123], 0\n\t"
    "v_sub_f32 v151, v79, v95\n\t"
    "v_cvt_pk_bf16_f32 v154, v148, v149\n\t"
    "v_mfma_f32_16x16x32_bf16 v[108:111], a[152:155], a[232:235], v[108:111]\n\t"
    "v_cvt_pk_bf16_f32 v155, v150, v151\n\t"
    "v_mfma_f32_16x16x32_bf16 v[108:111], a[156:159], a[236:239], v[108:111]\n\t"
    "ds_write_b128 v194, v[152:155] offset:28800\n\t"
    "v_mfma_f32_16x16x32_bf16 v[108:111], a[160:163], a[240:243], v[108:111]\n\t"
    "v_sub_f32 v144, v64, v104\n\t"
    "v_sub_f32 v145, v65, v105\n\t"
    "v_mfma_f32_16x16x32_bf16 v[92:95], a[166:169], a[132:135], 0\n\t"
    "v_sub_f32 v146, v66, v106\n\t"
    "v_sub_f32 v147, v67, v107\n\t"
    "v_mfma_f32_16x16x32_bf16 v[92:95], a[152:155], a[244:247], v[92:95]\n\t"
    "buffer_store_dwordx4 v[36:39], v200, s[52:55], s76 offen offset:0 sc1\n\t"
    "v_mfma_f32_16x16x32_bf16 v[92:95], a[156:159], a[248:251], v[92:95]\n\t"
    "v_fma_f32 v44, v44, s75, v32\n\t"
    "v_fma_f32 v45, v45, s75, v33\n\t"
    "v_mfma_f32_16x16x32_bf16 v[92:95], a[160:163], a[252:255], v[92:95]\n\t"
    "v_fma_f32 v46, v46, s75, v34\n\t"
    "v_fma_f32 v47, v47, s75, v35\n\t"
    "v_mfma_f32_16x16x32_bf16 v[64:67], a[164:167], a[124:127], 0\n\t"
    "v_cvt_pk_bf16_f32 v44, v44, v45\n\t"
    "v_cvt_pk_bf16_f32 v45, v46, v47\n\t"
    "v_mfma_f32_16x16x32_bf16 v[4:7], v[120:123], a[188:191], v[4:7]\n\t"
    "ds_write_b64 v210, v[44:45] offset:4096\n\t"
    "s_waitcnt lgkmcnt(2)\n\t"
    "v_mfma_f32_16x16x32_bf16 v[64:67], a[140:143], a[232:235], v[64:67]\n\t"
    "v_cvt_pk_bf16_f32 v156, v144, v145\n\t"
    "v_cvt_pk_bf16_f32 v157, v146, v147\n\t"
    "v_mfma_f32_16x16x32_bf16 v[64:67], a[144:147], a[236:239], v[64:67]\n\t"
    "ds_read_b128 a[152:155], v202 offset:15360\n\t"
    "v_sub_f32 v148, v68, v108\n\t"
    "v_mfma_f32_16x16x32_bf16 v[64:67], a[148:151], a[240:243], v[64:67]\n\t"
    "ds_read_b128 a[156:159], v202 offset:16384\n\t"
    "v_sub_f32 v149, v69, v109\n\t"
    "v_mfma_f32_16x16x32_bf16 v[4:7], v[132:135], a[192:195], v[4:7]\n\t"
    "ds_read_b128 a[160:163], v202 offset:17408\n\t"
    "v_sub_f32 v150, v70, v110\n\t"
    "v_mfma_f32_16x16x32_bf16 v[8:11], v[248:251], a[184:187], v[8:11]\n\t"
    "buffer_load_dwordx4 a[120:123], v199, s[40:43], s78 offen offset:64\n\t"
    "s_add_u32 s79, s73, 0x24000\n\t"
    "v_mfma_f32_16x16x32_bf16 v[8:11], v[124:127], a[188:191], v[8:11]\n\t"
    "ds_read_b128 a[184:187], v189 offset:41472\n\t"
    "v_sub_f32 v151, v71, v111\n\t"
    "v_mfma_f32_16x16x32_bf16 v[8:11], v[136:139], a[192:195], v[8:11]\n\t"
    "ds_read_b128 a[188:191], v189 offset:41536\n\t"
    "v_cvt_pk_bf16_f32 v158, v148, v149\n\t"
    "v_mfma_f32_16x16x32_bf16 v[0:3], v[140:143], a[196:199], v[0:3]\n\t"
    "ds_read_b128 a[192:195], v189 offset:41600\n\t"
    "v_cvt_pk_bf16_f32 v159, v150, v151\n\t"
    "v_mfma_f32_16x16x32_bf16 v[0:3], v[172:175], a[200:203], v[0:3]\n\t"
    "ds_write_b128 v194, v[156:159] offset:64\n\t"
    "v_mfma_f32_16x16x32_bf16 v[0:3], v[184:187], a[204:207], v[0:3]\n\t"
    "v_add_f32 v144, v112, v104\n\t"
    "v_add_f32 v145, v113, v105\n\t"
    "v_mfma_f32_16x16x32_bf16 v[4:7], v[164:167], a[196:199], v[4:7]\n\t"
    "v_add_f32 v146, v114, v106\n\t"
    "v_add_f32 v147, v115, v107\n\t"
    "v_mfma_f32_16x16x32_bf16 v[4:7], v[176:179], a[200:203], v[4:7]\n\t"
    "v_cvt_pk_bf16_f32 v160, v144, v145\n\t"
    "v_cvt_pk_bf16_f32 v161, v146, v147\n\t"
    "v_mfma_f32_16x16x32_bf16 v[4:7], v[48:51], a[204:207], v[4:7]\n\t"
    "ds_write_b128 v196, v[0:3] offset:36864\n\t"
    "v_mfma_f32_16x16x32_bf16 v[8:11], v[168:171], a[196:199], v[8:11]\n\t"
    "v_add_f32 v148, v116, v108\n\t"
    "v_add_f32 v149, v117, v109\n\t"
    "v_mfma_f32_16x16x32_bf16 v[8:11], v[180:183], a[200:203], v[8:11]\n\t"
    "v_add_f32 v150, v118, v110\n\t"
    "v_add_f32 v151, v119, v111\n\t"
    "v_mfma_f32_16x16x32_bf16 v[8:11], v[52:55], a[204:207], v[8:11]\n\t"
    "ds_write_b128 v196, v[4:7] offset:37888\n\t"
    "v_mfma_f32_16x16x32_bf16 v[12:15], a[108:111], a[172:175], 0\n\t"
    "v_cvt_pk_bf16_f32 v162, v148, v149\n\t"
    "v_cvt_pk_bf16_f32 v163, v150, v151\n\t"
    "v_mfma_f32_16x16x32_bf16 v[12:15], v[216:219], a[176:179], v[12:15]\n\t"
    "v_mfma_f32_16x16x32_bf16 v[12:15], v[228:231], a[180:183], v[12:15]\n\t"
    "ds_write_b128 v194, v[160:163] offset:9664\n\t"
    "v_mfma_f32_16x16x32_bf16 v[16:19], a[112:115], a[172:175], 0\n\t"
    "v_sub_f32 v144, v112, v104\n\t"
    "v_sub_f32 v145, v113, v105\n\t"
    "v_mfma_f32_16x16x32_bf16 v[16:19], v[220:223], a[176:179], v[16:19]\n\t"
    "v_sub_f32 v146, v114, v106\n\t"
    "v_sub_f32 v147, v115, v107\n\t"
    "v_mfma_f32_16x16x32_bf16 v[16:19], v[232:235], a[180:183], v[16:19]\n\t"
    "ds_write_b128 v196, v[8:11] offset:38912\n\t"
    "v_mfma_f32_16x16x32_bf16 v[20:23], v[212:215], a[172:175], 0\n\t"
    "v_cvt_pk_bf16_f32 v152, v144, v145\n\t"
    "v_cvt_pk_bf16_f32 v153, v146, v147\n\t"
    "v_mfma_f32_16x16x32_bf16 v[20:23], v[224:227], a[176:179], v[20:23]\n\t"
    "v_sub_f32 v148, v116, v108\n\t"
    "v_sub_f32 v149, v117, v109\n\t"
    "v_mfma_f32_16x16x32_bf16 v[20:23], v[236:239], a[180:183], v[20:23]\n\t"
    "ds_read_b128 a[196:199], v190 offset:41472\n\t"
    "v_sub_f32 v150, v118, v110\n\t"
    "s_waitcnt lgkmcnt(6)\n\t"
    "v_mfma_f32_16x16x32_bf16 v[12:15], v[240:243], a[184:187], v[12:15]\n\t"
    "ds_read_b128 a[200:203], v190 offset:41536\n\t"
    "v_sub_f32 v151, v119, v111\n\t"
    "v_mfma_f32_16x16x32_bf16 v[12:15], v[252:255], a[188:191], v[12:15]\n\t"
    "ds_read_b128 a[204:207], v190 offset:41600\n\t"
    "v_cvt_pk_bf16_f32 v154, v148, v149\n\t"
    "v_mfma_f32_16x16x32_bf16 v[12:15], v[128:131], a[192:195], v[12:15]\n\t"
    "ds_read_b128 a[172:175], v188 offset:44544\n\t"
    "v_cvt_pk_bf16_f32 v155, v150, v151\n\t"
    "v_mfma_f32_16x16x32_bf16 v[16:19], v[244:247], a[184:187], v[16:19]\n\t"
    "ds_write_b128 v194, v[152:155] offset:19264\n\t"
    "v_mfma_f32_16x16x32_bf16 v[16:19], v[120:123], a[188:191], v[16:19]\n\t"
    "v_sub_f32 v144, v88, v112\n\t"
    "v_sub_f32 v145, v89, v113\n\t"
    "v_mfma_f32_16x16x32_bf16 v[16:19], v[132:135], a[192:195], v[16:19]\n\t"
    "v_sub_f32 v146, v90, v114\n\t"
    "v_sub_f32 v147, v91, v115\n\t"
    "s_waitcnt vmcnt(10)\n\t"
    "v_mfma_f32_16x16x32_bf16 v[112:115], a[164:167], a[136:139], 0\n\t"
    "ds_read_b128 a[176:179], v188 offset:44608\n\t"
    "v_cvt_pk_bf16_f32 v156, v144, v145\n\t"
    "v_mfma_f32_16x16x32_bf16 v[112:115], a[140:143], a[244:247], v[112:115]\n\t"
    "ds_read_b128 a[180:183], v188 offset:44672\n\t"
    "v_cvt_pk_bf16_f32 v157, v146, v147\n\t"
    "v_mfma_f32_16x16x32_bf16 v[112:115], a[144:147], a[248:251], v[112:115]\n\t"
    "ds_read_b128 a[140:143], v202 offset:0\n\t"
    "v_sub_f32 v148, v92, v116\n\t"
    "v_mfma_f32_16x16x32_bf16 v[112:115], a[148:151], a[252:255], v[112:115]\n\t"
    "ds_read_b128 a[144:147], v202 offset:1024\n\t"
    "v_sub_f32 v149, v93, v117\n\t"
    "v_mfma_f32_16x16x32_bf16 v[68:71], a[166:169], a[124:127], 0\n\t"
    "buffer_load_dwordx4 a[124:127], v199, s[40:43], s79 offen offset:128\n\t"
    "s_add_u32 s80, s70, 0xa2000\n\t"
    "v_mfma_f32_16x16x32_bf16 v[68:71], a[152:155], a[232:235], v[68:71]\n\t"
    "buffer_load_dwordx4 a[232:235], v198, s[36:39], s80 offen offset:0\n\t"
    "v_mfma_f32_16x16x32_bf16 v[68:71], a[156:159], a[236:239], v[68:71]\n\t"
    "buffer_load_dwordx4 a[236:239], v198, s[36:39], s80 offen offset:64\n\t"
    "v_mfma_f32_16x16x32_bf16 v[68:71], a[160:163], a[240:243], v[68:71]\n\t"
    "ds_read_b128 a[148:151], v202 offset:2048\n\t"
    "v_sub_f32 v150, v94, v118\n\t"
    "v_mfma_f32_16x16x32_bf16 v[20:23], v[248:251], a[184:187], v[20:23]\n\t"
    "ds_read_b128 a[184:187], v189 offset:44544\n\t"
    "v_sub_f32 v151, v95, v119\n\t"
    "v_mfma_f32_16x16x32_bf16 v[116:119], a[166:169], a[136:139], 0\n\t"
    "buffer_load_dwordx4 a[240:243], v198, s[36:39], s80 offen offset:128\n\t"
    "s_add_u32 s81, s70, 0xb4000\n\t"
    "v_mfma_f32_16x16x32_bf16 v[116:119], a[152:155], a[244:247], v[116:119]\n\t"
    "ds_read_b128 a[152:155], v202 offset:3072\n\t"
    "v_cvt_pk_bf16_f32 v158, v148, v149\n\t"
    "v_mfma_f32_16x16x32_bf16 v[116:119], a[156:159], a[248:251], v[116:119]\n\t"
    "ds_read_b128 a[156:159], v202 offset:4096\n\t"
    "v_cvt_pk_bf16_f32 v159, v150, v151\n\t"
    "v_mfma_f32_16x16x32_bf16 v[116:119], a[160:163], a[252:255], v[116:119]\n\t"
    "ds_write_b128 v194, v[156:159] offset:28864\n\t"
    "v_mfma_f32_16x16x32_bf16 v[20:23], v[124:127], a[188:191], v[20:23]\n\t"
    "v_sub_f32 v144, v80, v64\n\t"
    "v_sub_f32 v145, v81, v65\n\t"
    "v_mfma_f32_16x16x32_bf16 v[20:23], v[136:139], a[192:195], v[20:23]\n\t"
    "v_sub_f32 v146, v82, v66\n\t"
    "v_sub_f32 v147, v83, v67\n\t"
    "v_mfma_f32_16x16x32_bf16 v[80:83], a[164:167], a[128:131], 0\n\t"
    "ds_read_b128 a[188:191], v189 offset:44608\n\t"
    "v_cvt_pk_bf16_f32 v160, v144, v145\n\t"
    "s_waitcnt lgkmcnt(12)\n\t"
    "v_mfma_f32_16x16x32_bf16 v[12:15], v[140:143], a[196:199], v[12:15]\n\t"
    "ds_read_b128 a[192:195], v189 offset:44672\n\t"
    "v_cvt_pk_bf16_f32 v161, v146, v147\n\t"
    "v_mfma_f32_16x16x32_bf16 v[12:15], v[172:175], a[200:203], v[12:15]\n\t"
    "ds_read_b128 a[160:163], v202 offset:5120\n\t"
    "v_sub_f32 v148, v84, v68\n\t"
    "s_waitcnt vmcnt(11) lgkmcnt(7)\n\t"
    "v_mfma_f32_16x16x32_bf16 v[80:83], a[140:143], a[208:211], v[80:83]\n\t"
    "buffer_load_dwordx4 a[244:247], v198, s[36:39], s81 offen offset:0\n\t"
    "v_mfma_f32_16x16x32_bf16 v[80:83], a[144:147], a[212:215], v[80:83]\n\t"
    "v_sub_f32 v149, v85, v69\n\t"
    "v_sub_f32 v150, v86, v70\n\t"
    "v_mfma_f32_16x16x32_bf16 v[80:83], a[148:151], a[216:219], v[80:83]\n\t"
    "v_sub_f32 v151, v87, v71\n\t"
    "v_cvt_pk_bf16_f32 v162, v148, v149\n\t"
    "v_mfma_f32_16x16x32_bf16 v[12:15], v[184:187], a[204:207], v[12:15]\n\t"
    "v_cvt_pk_bf16_f32 v163, v150, v151\n\t"
    "v_mfma_f32_16x16x32_bf16 v[16:19], v[164:167], a[196:199], v[16:19]\n\t"
    "ds_write_b128 v194, v[160:163] offset:128\n\t"
    "v_mfma_f32_16x16x32_bf16 v[16:19], v[176:179], a[200:203], v[16:19]\n\t"
    "buffer_load_dwordx4 a[248:251], v198, s[36:39], s81 offen offset:64\n\t"
    "v_mfma_f32_16x16x32_bf16 v[16:19], v[48:51], a[204:207], v[16:19]\n\t"
    "v_add_f32 v144, v56, v64\n\t"
    "v_add_f32 v145, v57, v65\n\t"
    "v_mfma_f32_16x16x32_bf16 v[20:23], v[168:171], a[196:199], v[20:23]\n\t"
    "ds_write_b128 v196, v[12:15] offset:0\n\t"
    "v_mfma_f32_16x16x32_bf16 v[20:23], v[180:183], a[200:203], v[20:23]\n\t"
    "buffer_load_dwordx4 a[252:255], v198, s[36:39], s81 offen offset:128\n\t"
    "s_add_u32 s78, s73, 0x2d000\n\t"
    "v_mfma_f32_16x16x32_bf16 v[20:23], v[52:55], a[204:207], v[20:23]\n\t"
    "v_add_f32 v146, v58, v66\n\t"
    "v_add_f32 v147, v59, v67\n\t"
    "v_mfma_f32_16x16x32_bf16 v[24:27], a[108:111], a[172:175], 0\n\t"
    "ds_write_b128 v196, v[16:19] offset:1024\n\t"
    "v_mfma_f32_16x16x32_bf16 v[24:27], v[216:219], a[176:179], v[24:27]\n\t"
    "v_cvt_pk_bf16_f32 v152, v144, v145\n\t"
    "v_cvt_pk_bf16_f32 v153, v146, v147\n\t"
    "v_mfma_f32_16x16x32_bf16 v[24:27], v[228:231], a[180:183], v[24:27]\n\t"
    "v_add_f32 v148, v60, v68\n\t"
    "v_add_f32 v149, v61, v69\n\t"
    "v_mfma_f32_16x16x32_bf16 v[28:31], a[112:115], a[172:175], 0\n\t"
    "ds_write_b128 v196, v[20:23] offset:2048\n\t"
    "v_mfma_f32_16x16x32_bf16 v[28:31], v[220:223], a[176:179], v[28:31]\n\t"
    "v_add_f32 v150, v62, v70\n\t"
    "v_add_f32 v151, v63, v71\n\t"
    "v_mfma_f32_16x16x32_bf16 v[28:31], v[232:235], a[180:183], v[28:31]\n\t"
    "v_cvt_pk_bf16_f32 v154, v148, v149\n\t"
    "v_cvt_pk_bf16_f32 v155, v150, v151\n\t"
    "v_mfma_f32_16x16x32_bf16 v[32:35], v[212:215], a[172:175], 0\n\t"
    "ds_write_b128 v194, v[152:155] offset:9728\n\t"
    "v_mfma_f32_16x16x32_bf16 v[32:35], v[224:227], a[176:179], v[32:35]\n\t"
    "v_sub_f32 v144, v56, v64\n\t"
    "v_sub_f32 v145, v57, v65\n\t"
    "v_mfma_f32_16x16x32_bf16 v[32:35], v[236:239], a[180:183], v[32:35]\n\t"
    "v_sub_f32 v146, v58, v66\n\t"
    "v_sub_f32 v147, v59, v67\n\t"
    "s_waitcnt lgkmcnt(6)\n\t"
    "v_mfma_f32_16x16x32_bf16 v[24:27], v[240:243], a[184:187], v[24:27]\n\t"
    "ds_read_b128 a[196:199], v190 offset:44544\n\t"
    "v_cvt_pk_bf16_f32 v156, v144, v145\n\t"
    "v_mfma_f32_16x16x32_bf16 v[24:27], v[252:255], a[188:191], v[24:27]\n\t"
    "ds_read_b128 a[200:203], v190 offset:44608\n\t"
    "v_cvt_pk_bf16_f32 v157, v146, v147\n\t"
    "v_mfma_f32_16x16x32_bf16 v[24:27], v[128:131], a[192:195], v[24:27]\n\t"
    "ds_read_b128 a[204:207], v190 offset:44672\n\t"
    "v_sub_f32 v148, v60, v68\n\t"
    "v_mfma_f32_16x16x32_bf16 v[28:31], v[244:247], a[184:187], v[28:31]\n\t"
    "ds_read_b128 a[172:175], v191 offset:38400\n\t"
    "v_sub_f32 v149, v61, v69\n\t"
    "v_mfma_f32_16x16x32_bf16 v[28:31], v[120:123], a[188:191], v[28:31]\n\t"
    "ds_read_b128 a[176:179], v191 offset:38464\n\t"
    "v_sub_f32 v150, v62, v70\n\t"
    "v_mfma_f32_16x16x32_bf16 v[28:31], v[132:135], a[192:195], v[28:31]\n\t"
    "ds_read_b128 a[180:183], v191 offset:38528\n\t"
    "v_sub_f32 v151, v63, v71\n\t"
    "v_mfma_f32_16x16x32_bf16 v[32:35], v[248:251], a[184:187], v[32:35]\n\t"
    "ds_read_b128 a[184:187], v192 offset:38400\n\t"
    "v_cvt_pk_bf16_f32 v158, v148, v149\n\t"
    "v_mfma_f32_16x16x32_bf16 v[32:35], v[124:127], a[188:191], v[32:35]\n\t"
    "ds_read_b128 a[188:191], v192 offset:38464\n\t"
    "v_cvt_pk_bf16_f32 v159, v150, v151\n\t"
    "v_mfma_f32_16x16x32_bf16 v[32:35], v[136:139], a[192:195], v[32:35]\n\t"
    "ds_write_b128 v194, v[156:159] offset:19328\n\t"
    "v_sub_f32 v144, v112, v56\n\t"
    "v_sub_f32 v145, v113, v57\n\t"
    "v_sub_f32 v146, v114, v58\n\t"
    "v_sub_f32 v147, v115, v59\n\t"
    "s_waitcnt vmcnt(10)\n\t"
    "v_mfma_f32_16x16x32_bf16 v[56:59], a[164:167], a[116:119], 0\n\t"
    "v_cvt_pk_bf16_f32 v160, v144, v145\n\t"
    "v_cvt_pk_bf16_f32 v161, v146, v147\n\t"
    "v_mfma_f32_16x16x32_bf16 v[56:59], a[140:143], a[220:223], v[56:59]\n\t"
    "ds_read_b128 a[192:195], v192 offset:38528\n\t"
    "v_sub_f32 v148, v116, v60\n\t"
    "v_mfma_f32_16x16x32_bf16 v[56:59], a[144:147], a[224:227], v[56:59]\n\t"
    "ds_read_b128 a[140:143], v202 offset:6144\n\t"
    "v_sub_f32 v149, v117, v61\n\t"
    "v_mfma_f32_16x16x32_bf16 v[56:59], a[148:151], a[228:231], v[56:59]\n\t"
    "ds_read_b128 a[144:147], v202 offset:7168\n\t"
    "v_sub_f32 v150, v118, v62\n\t"
    "v_mfma_f32_16x16x32_bf16 v[84:87], a[166:169], a[128:131], 0\n\t"
    "buffer_load_dwordx4 a[128:131], v199, s[40:43], s78 offen offset:0\n\t"
    "s_add_u32 s79, s73, 0x2d000\n\t"
    "s_waitcnt lgkmcnt(15)\n\t"
    "v_mfma_f32_16x16x32_bf16 v[84:87], a[152:155], a[208:211], v[84:87]\n\t"
    "ds_read_b128 a[148:151], v202 offset:8192\n\t"
    "v_sub_f32 v151, v119, v63\n\t"
    "v_mfma_f32_16x16x32_bf16 v[84:87], a[156:159], a[212:215], v[84:87]\n\t"
    "v_cvt_pk_bf16_f32 v162, v148, v149\n\t"
    "v_cvt_pk_bf16_f32 v163, v150, v151\n\t"
    "v_mfma_f32_16x16x32_bf16 v[84:87], a[160:163], a[216:219], v[84:87]\n\t"
    "ds_write_b128 v194, v[160:163] offset:28928\n\t"
    "v_mfma_f32_16x16x32_bf16 v[60:63], a[166:169], a[116:119], 0\n\t"
    "v_sub_f32 v144, v96, v80\n\t"
    "v_sub_f32 v145, v97, v81\n\t"
    "v_mfma_f32_16x16x32_bf16 v[60:63], a[152:155], a[220:223], v[60:63]\n\t"
    "v_sub_f32 v146, v98, v82\n\t"
    "v_sub_f32 v147, v99, v83\n\t"
    "v_mfma_f32_16x16x32_bf16 v[60:63], a[156:159], a[224:227], v[60:63]\n\t"
    "ds_read_b128 a[152:155], v202 offset:9216\n\t"
    "v_cvt_pk_bf16_f32 v152, v144, v145\n\t"
    "v_mfma_f32_16x16x32_bf16 v[60:63], a[160:163], a[228:231], v[60:63]\n\t"
    "ds_read_b128 a[156:159], v202 offset:10240\n\t"
    "v_cvt_pk_bf16_f32 v153, v146, v147\n\t"
    "v_mfma_f32_16x16x32_bf16 v[96:99], a[164:167], a[132:135], 0\n\t"
    "ds_read_b128 a[160:163], v202 offset:11264\n\t"
    "v_sub_f32 v148, v100, v84\n\t"
    "s_waitcnt lgkmcnt(14)\n\t"
    "v_mfma_f32_16x16x32_bf16 v[24:27], v[140:143], a[196:199], v[24:27]\n\t"
    "v_sub_f32 v149, v101, v85\n\t"
    "v_sub_f32 v150, v102, v86\n\t"
    "v_mfma_f32_16x16x32_bf16 v[24:27], v[172:175], a[200:203], v[24:27]\n\t"
    "v_sub_f32 v151, v103, v87\n\t"
    "v_cvt_pk_bf16_f32 v154, v148, v149\n\t"
    "v_mfma_f32_16x16x32_bf16 v[24:27], v[184:187], a[204:207], v[24:27]\n\t"
    "v_cvt_pk_bf16_f32 v155, v150, v151\n\t"
    "s_waitcnt lgkmcnt(4)\n\t"
    "v_mfma_f32_16x16x32_bf16 v[96:99], a[140:143], a[208:211], v[96:99]\n\t"
    "v_mfma_f32_16x16x32_bf16 v[96:99], a[144:147], a[212:215], v[96:99]\n\t"
    "v_mfma_f32_16x16x32_bf16 v[96:99], a[148:151], a[216:219], v[96:99]\n\t"
    "v_mfma_f32_16x16x32_bf16 v[28:31], v[164:167], a[196:199], v[28:31]\n\t"
    "v_mfma_f32_16x16x32_bf16 v[28:31], v[176:179], a[200:203], v[28:31]\n\t"
    "v_mfma_f32_16x16x32_bf16 v[28:31], v[48:51], a[204:207], v[28:31]\n\t"
    "v_mfma_f32_16x16x32_bf16 v[32:35], v[168:171], a[196:199], v[32:35]\n\t"
    "ds_write_b128 v196, v[24:27] offset:12288\n\t"
    "v_mfma_f32_16x16x32_bf16 v[32:35], v[180:183], a[200:203], v[32:35]\n\t"
    "v_mfma_f32_16x16x32_bf16 v[32:35], v[52:55], a[204:207], v[32:35]\n\t"
    "v_mfma_f32_16x16x32_bf16 v[0:3], a[0:3], a[172:175], v[0:3]\n\t"
    "ds_read_b128 a[196:199], v193 offset:38400\n\t"
    "v_mfma_f32_16x16x32_bf16 v[0:3], a[12:15], a[176:179], v[0:3]\n\t"
    "ds_read_b128 a[200:203], v193 offset:38464\n\t"
    "v_mfma_f32_16x16x32_bf16 v[0:3], a[24:27], a[180:183], v[0:3]\n\t"
    "ds_write_b128 v196, v[28:31] offset:13312\n\t"
    "v_mfma_f32_16x16x32_bf16 v[4:7], a[4:7], a[172:175], v[4:7]\n\t"
    "v_mfma_f32_16x16x32_bf16 v[4:7], a[16:19], a[176:179], v[4:7]\n\t"
    "v_mfma_f32_16x16x32_bf16 v[4:7], a[28:31], a[180:183], v[4:7]\n\t"
    "ds_write_b128 v196, v[32:35] offset:14336\n\t"
    "v_mfma_f32_16x16x32_bf16 v[8:11], a[8:11], a[172:175], v[8:11]\n\t"
    "v_mfma_f32_16x16x32_bf16 v[8:11], a[20:23], a[176:179], v[8:11]\n\t"
    "v_mfma_f32_16x16x32_bf16 v[8:11], a[32:35], a[180:183], v[8:11]\n\t"
    "ds_read_b128 a[204:207], v193 offset:38528\n\t"
    "ds_read_b128 a[172:175], v191 offset:41472\n\t"
    "ds_read_b128 a[176:179], v191 offset:41536\n\t"
    "ds_read_b128 a[180:183], v191 offset:41600\n\t"
    "s_waitcnt lgkmcnt(4)\n\t"
    "s_barrier\n\t"
    "v_mfma_f32_16x16x32_bf16 v[0:3], a[36:39], a[184:187], v[0:3]\n\t"
    "ds_read_b128 v[36:39], v197 offset:36864\n\t"
    "v_mfma_f32_16x16x32_bf16 v[0:3], a[48:51], a[188:191], v[0:3]\n\t"
    "ds_read_b128 v[40:43], v197 offset:37888\n\t"
    "v_mfma_f32_16x16x32_bf16 v[0:3], a[60:63], a[192:195], v[0:3]\n\t"
    "ds_read_b128 v[44:47], v197 offset:38912\n\t"
    "s_add_u32 s77, s74, 0x24000\n\t"
    "v_mfma_f32_16x16x32_bf16 v[4:7], a[40:43], a[184:187], v[4:7]\n\t"
    "ds_read_b64 a[170:171], v211 offset:2048\n\t"
    "s_add_u32 s86, s74, 0xc00\n\t"
    "v_mfma_f32_16x16x32_bf16 v[4:7], a[52:55], a[188:191], v[4:7]\n\t"
    "v_mfma_f32_16x16x32_bf16 v[4:7], a[64:67], a[192:195], v[4:7]\n\t"
    "v_mfma_f32_16x16x32_bf16 v[8:11], a[44:47], a[184:187], v[8:11]\n\t"
    "ds_read_b128 a[184:187], v192 offset:41472\n\t"
    "v_mfma_f32_16x16x32_bf16 v[8:11], a[56:59], a[188:191], v[8:11]\n\t"
    "ds_read_b128 a[188:191], v192 offset:41536\n\t"
    "v_mfma_f32_16x16x32_bf16 v[8:11], a[68:71], a[192:195], v[8:11]\n\t"
    "ds_read_b128 a[192:195], v192 offset:41600\n\t"
    "s_waitcnt lgkmcnt(10)\n\t"
    "v_mfma_f32_16x16x32_bf16 v[0:3], a[72:75], a[196:199], v[0:3]\n\t"
    "v_mfma_f32_16x16x32_bf16 v[0:3], a[84:87], a[200:203], v[0:3]\n\t"
    "v_mfma_f32_16x16x32_bf16 v[0:3], a[96:99], a[204:207], v[0:3]\n\t"
    "v_mfma_f32_16x16x32_bf16 v[4:7], a[76:79], a[196:199], v[4:7]\n\t"
    "v_mfma_f32_16x16x32_bf16 v[4:7], a[88:91], a[200:203], v[4:7]\n\t"
    "v_mfma_f32_16x16x32_bf16 v[4:7], a[100:103], a[204:207], v[4:7]\n\t"
    "v_mfma_f32_16x16x32_bf16 v[8:11], a[80:83], a[196:199], v[8:11]\n\t"
    "s_waitcnt lgkmcnt(3)\n\t"
    "buffer_store_dwordx2 a[170:171], v201, s[52:55], s86 offen offset:0 sc1\n\t"
    "v_mfma_f32_16x16x32_bf16 v[8:11], a[92:95], a[200:203], v[8:11]\n\t"
    "ds_read_b128 a[196:199], v193 offset:41472\n\t"
    "v_mfma_f32_16x16x32_bf16 v[8:11], a[104:107], a[204:207], v[8:11]\n\t"
    "ds_read_b128 a[200:203], v193 offset:41536\n\t"
    "v_fma_f32 v36, v36, s75, v0\n\t"
    "v_mfma_f32_16x16x32_bf16 v[12:15], a[0:3], a[172:175], v[12:15]\n\t"
    "ds_read_b128 a[204:207], v193 offset:41600\n\t"
    "v_fma_f32 v37, v37, s75, v1\n\t"
    "v_mfma_f32_16x16x32_bf16 v[12:15], a[12:15], a[176:179], v[12:15]\n\t"
    "v_fma_f32 v38, v38, s75, v2\n\t"
    "v_fma_f32 v39, v39, s75, v3\n\t"
    "v_mfma_f32_16x16x32_bf16 v[12:15], a[24:27], a[180:183], v[12:15]\n\t"
    "v_cvt_pk_bf16_f32 v36, v36, v37\n\t"
    "v_cvt_pk_bf16_f32 v37, v38, v39\n\t"
    "v_mfma_f32_16x16x32_bf16 v[16:19], a[4:7], a[172:175], v[16:19]\n\t"
    "v_fma_f32 v40, v40, s75, v4\n\t"
    "v_fma_f32 v41, v41, s75, v5\n\t"
    "v_mfma_f32_16x16x32_bf16 v[16:19], a[16:19], a[176:179], v[16:19]\n\t"
    "v_fma_f32 v42, v42, s75, v6\n\t"
    "v_fma_f32 v43, v43, s75, v7\n\t"
    "v_mfma_f32_16x16x32_bf16 v[16:19], a[28:31], a[180:183], v[16:19]\n\t"
    "v_cvt_pk_bf16_f32 v38, v40, v41\n\t"
    "v_cvt_pk_bf16_f32 v39, v42, v43\n\t"
    "v_mfma_f32_16x16x32_bf16 v[20:23], a[8:11], a[172:175], v[20:23]\n\t"
    "buffer_store_dwordx4 v[36:39], v200, s[52:55], s77 offen offset:0 sc1\n\t"
    "v_mfma_f32_16x16x32_bf16 v[20:23], a[20:23], a[176:179], v[20:23]\n\t"
    "ds_read_b128 a[172:175], v191 offset:44544\n\t"
    "v_fma_f32 v44, v44, s75, v8\n\t"
    "v_mfma_f32_16x16x32_bf16 v[20:23], a[32:35], a[180:183], v[20:23]\n\t"
    "ds_read_b128 a[176:179], v191 offset:44608\n\t"
    "v_fma_f32 v45, v45, s75, v9\n\t"
    "s_waitcnt lgkmcnt(5)\n\t"
    "v_mfma_f32_16x16x32_bf16 v[12:15], a[36:39], a[184:187], v[12:15]\n\t"
    "ds_read_b128 a[180:183], v191 offset:44672\n\t"
    "v_fma_f32 v46, v46, s75, v10\n\t"
    "v_mfma_f32_16x16x32_bf16 v[12:15], a[48:51], a[188:191], v[12:15]\n\t"
    "v_fma_f32 v47, v47, s75, v11\n\t"
    "v_cvt_pk_bf16_f32 v44, v44, v45\n\t"
    "v_mfma_f32_16x16x32_bf16 v[12:15], a[60:63], a[192:195], v[12:15]\n\t"
    "v_cvt_pk_bf16_f32 v45, v46, v47\n\t"
    "ds_read_b64 a[170:171], v211 offset:4096\n\t"
    "s_add_u32 s86, s74, 0x1800\n\t"
    "v_mfma_f32_16x16x32_bf16 v[16:19], a[40:43], a[184:187], v[16:19]\n\t"
    "ds_write_b64 v210, v[44:45] offset:6144\n\t"
    "v_mfma_f32_16x16x32_bf16 v[16:19], a[52:55], a[188:191], v[16:19]\n\t"
    "v_mfma_f32_16x16x32_bf16 v[16:19], a[64:67], a[192:195], v[16:19]\n\t"
    "ds_read_b128 v[36:39], v197 offset:0\n\t"
    "v_mfma_f32_16x16x32_bf16 v[20:23], a[44:47], a[184:187], v[20:23]\n\t"
    "ds_read_b128 a[184:187], v192 offset:44544\n\t"
    "v_mfma_f32_16x16x32_bf16 v[20:23], a[56:59], a[188:191], v[20:23]\n\t"
    "ds_read_b128 a[188:191], v192 offset:44608\n\t"
    "v_mfma_f32_16x16x32_bf16 v[20:23], a[68:71], a[192:195], v[20:23]\n\t"
    "ds_read_b128 a[192:195], v192 offset:44672\n\t"
    "s_waitcnt lgkmcnt(9)\n\t"
    "v_mfma_f32_16x16x32_bf16 v[12:15], a[72:75], a[196:199], v[12:15]\n\t"
    "ds_read_b128 v[40:43], v197 offset:1024\n\t"
    "v_mfma_f32_16x16x32_bf16 v[12:15], a[84:87], a[200:203], v[12:15]\n\t"
    "ds_read_b128 v[44:47], v197 offset:2048\n\t"
    "s_add_u32 s76, s74, 0x24c00\n\t"
    "v_mfma_f32_16x16x32_bf16 v[12:15], a[96:99], a[204:207], v[12:15]\n\t"
    "v_mfma_f32_16x16x32_bf16 v[16:19], a[76:79], a[196:199], v[16:19]\n\t"
    "v_mfma_f32_16x16x32_bf16 v[16:19], a[88:91], a[200:203], v[16:19]\n\t"
    "v_mfma_f32_16x16x32_bf16 v[16:19], a[100:103], a[204:207], v[16:19]\n\t"
    "s_waitcnt lgkmcnt(7)\n\t"
    "buffer_store_dwordx2 a[170:171], v201, s[52:55], s86 offen offset:0 sc1\n\t"
    "v_mfma_f32_16x16x32_bf16 v[20:23], a[80:83], a[196:199], v[20:23]\n\t"
    "ds_read_b128 a[196:199], v193 offset:44544\n\t"
    "v_mfma_f32_16x16x32_bf16 v[20:23], a[92:95], a[200:203], v[20:23]\n\t"
    "ds_read_b128 a[200:203], v193 offset:44608\n\t"
    "v_mfma_f32_16x16x32_bf16 v[20:23], a[104:107], a[204:207], v[20:23]\n\t"
    "ds_read_b128 a[204:207], v193 offset:44672\n\t"
    "v_mfma_f32_16x16x32_bf16 v[24:27], a[0:3], a[172:175], v[24:27]\n\t"
    "v_mfma_f32_16x16x32_bf16 v[24:27], a[12:15], a[176:179], v[24:27]\n\t"
    "v_mfma_f32_16x16x32_bf16 v[24:27], a[24:27], a[180:183], v[24:27]\n\t"
    "v_mfma_f32_16x16x32_bf16 v[28:31], a[4:7], a[172:175], v[28:31]\n\t"
    "s_waitcnt lgkmcnt(3)\n\t"
    "v_fma_f32 v36, v36, s75, v12\n\t"
    "v_mfma_f32_16x16x32_bf16 v[28:31], a[16:19], a[176:179], v[28:31]\n\t"
    "v_fma_f32 v37, v37, s75, v13\n\t"
    "v_fma_f32 v38, v38, s75, v14\n\t"
    "v_mfma_f32_16x16x32_bf16 v[28:31], a[28:31], a[180:183], v[28:31]\n\t"
    "v_fma_f32 v39, v39, s75, v15\n\t"
    "v_cvt_pk_bf16_f32 v36, v36, v37\n\t"
    "v_mfma_f32_16x16x32_bf16 v[32:35], a[8:11], a[172:175], v[32:35]\n\t"
    "v_cvt_pk_bf16_f32 v37, v38, v39\n\t"
    "v_fma_f32 v40, v40, s75, v16\n\t"
    "v_mfma_f32_16x16x32_bf16 v[32:35], a[20:23], a[176:179], v[32:35]\n\t"
    "v_fma_f32 v41, v41, s75, v17\n\t"
    "v_fma_f32 v42, v42, s75, v18\n\t"
    "v_mfma_f32_16x16x32_bf16 v[32:35], a[32:35], a[180:183], v[32:35]\n\t"
    "v_fma_f32 v43, v43, s75, v19\n\t"
    "v_cvt_pk_bf16_f32 v38, v40, v41\n\t"
    "v_cvt_pk_bf16_f32 v39, v42, v43\n\t"
    "buffer_store_dwordx4 v[36:39], v200, s[52:55], s76 offen offset:0 sc1\n\t"
    "v_fma_f32 v44, v44, s75, v20\n\t"
    "s_waitcnt lgkmcnt(0)\n\t"
    "s_barrier\n\t"
    "v_mfma_f32_16x16x32_bf16 v[24:27], a[36:39], a[184:187], v[24:27]\n\t"
    "ds_write_b128 v195, v[152:155] offset:0\n\t"
    "v_mfma_f32_16x16x32_bf16 v[24:27], a[48:51], a[188:191], v[24:27]\n\t"
    "v_fma_f32 v45, v45, s75, v21\n\t"
    "v_fma_f32 v46, v46, s75, v22\n\t"
    "v_mfma_f32_16x16x32_bf16 v[24:27], a[60:63], a[192:195], v[24:27]\n\t"
    "v_fma_f32 v47, v47, s75, v23\n\t"
    "v_cvt_pk_bf16_f32 v44, v44, v45\n\t"
    "v_mfma_f32_16x16x32_bf16 v[28:31], a[40:43], a[184:187], v[28:31]\n\t"
    "ds_read_b128 a[172:175], v188 offset:0\n\t"
    "v_cvt_pk_bf16_f32 v45, v46, v47\n\t"
    "v_mfma_f32_16x16x32_bf16 v[28:31], a[52:55], a[188:191], v[28:31]\n\t"
    "ds_write_b64 v210, v[44:45] offset:8192\n\t"
    "v_mfma_f32_16x16x32_bf16 v[28:31], a[64:67], a[192:195], v[28:31]\n\t"
    "v_add_f32 v144, v72, v80\n\t"
    "v_add_f32 v145, v73, v81\n\t"
    "v_mfma_f32_16x16x32_bf16 v[32:35], a[44:47], a[184:187], v[32:35]\n\t"
    "ds_read_b128 a[176:179], v188 offset:64\n\t"
    "v_add_f32 v146, v74, v82\n\t"
    "v_mfma_f32_16x16x32_bf16 v[32:35], a[56:59], a[188:191], v[32:35]\n\t"
    "ds_read_b128 a[180:183], v188 offset:128\n\t"
    "v_add_f32 v147, v75, v83\n\t"
    "v_mfma_f32_16x16x32_bf16 v[32:35], a[68:71], a[192:195], v[32:35]\n\t"
    "ds_read_b128 a[184:187], v189 offset:0\n\t"
    "v_cvt_pk_bf16_f32 v156, v144, v145\n\t"
    "v_mfma_f32_16x16x32_bf16 v[24:27], a[72:75], a[196:199], v[24:27]\n\t"
    "ds_read_b128 a[188:191], v189 offset:64\n\t"
    "v_cvt_pk_bf16_f32 v157, v146, v147\n\t"
    "v_mfma_f32_16x16x32_bf16 v[24:27], a[84:87], a[200:203], v[24:27]\n\t"
    "ds_read_b128 a[192:195], v189 offset:128\n\t"
    "v_add_f32 v148, v76, v84\n\t"
    "v_mfma_f32_16x16x32_bf16 v[24:27], a[96:99], a[204:207], v[24:27]\n\t"
    "ds_read_b128 v[36:39], v197 offset:12288\n\t"
    "v_add_f32 v149, v77, v85\n\t"
    "v_mfma_f32_16x16x32_bf16 v[28:31], a[76:79], a[196:199], v[28:31]\n\t"
    "ds_read_b128 v[40:43], v197 offset:13312\n\t"
    "v_add_f32 v150, v78, v86\n\t"
    "v_mfma_f32_16x16x32_bf16 v[28:31], a[88:91], a[200:203], v[28:31]\n\t"
    "ds_read_b128 v[44:47], v197 offset:14336\n\t"
    "s_add_u32 s77, s74, 0x25800\n\t"
    "v_add_f32 v151, v79, v87\n\t"
    "v_mfma_f32_16x16x32_bf16 v[28:31], a[100:103], a[204:207], v[28:31]\n\t"
    "ds_read_b64 a[170:171], v211 offset:6144\n\t"
    "s_add_u32 s86, s74, 0x24000\n\t"
    "v_cvt_pk_bf16_f32 v158, v148, v149\n\t"
    "v_mfma_f32_16x16x32_bf16 v[32:35], a[80:83], a[196:199], v[32:35]\n\t"
    "ds_read_b128 a[196:199], v190 offset:0\n\t"
    "v_cvt_pk_bf16_f32 v159, v150, v151\n\t"
    "v_mfma_f32_16x16x32_bf16 v[32:35], a[92:95], a[200:203], v[32:35]\n\t"
    "ds_write_b128 v195, v[156:159] offset:9600\n\t"
    "v_mfma_f32_16x16x32_bf16 v[32:35], a[104:107], a[204:207], v[32:35]\n\t"
    "v_sub_f32 v144, v72, v80\n\t"
    "v_sub_f32 v145, v73, v81\n\t"
    "v_sub_f32 v146, v74, v82\n\t"
    "s_waitcnt lgkmcnt(9)\n\t"
    "v_mfma_f32_16x16x32_bf16 v[0:3], a[108:111], a[172:175], 0\n\t"
    "v_sub_f32 v147, v75, v83\n\t"
    "v_cvt_pk_bf16_f32 v160, v144, v145\n\t"
    "v_mfma_f32_16x16x32_bf16 v[0:3], v[216:219], a[176:179], v[0:3]\n\t"
    "ds_read_b128 a[200:203], v190 offset:64\n\t"
    "v_cvt_pk_bf16_f32 v161, v146, v147\n\t"
    "v_mfma_f32_16x16x32_bf16 v[0:3], v[228:231], a[180:183], v[0:3]\n\t"
    "ds_read_b128 a[204:207], v190 offset:128\n\t"
    "v_sub_f32 v148, v76, v84\n\t"
    "v_mfma_f32_16x16x32_bf16 v[4:7], a[112:115], a[172:175], 0\n\t"
    "v_sub_f32 v149, v77, v85\n\t"
    "v_sub_f32 v150, v78, v86\n\t"
    "v_mfma_f32_16x16x32_bf16 v[4:7], v[220:223], a[176:179], v[4:7]\n\t"
    "v_sub_f32 v151, v79, v87\n\t"
    "v_cvt_pk_bf16_f32 v162, v148, v149\n\t"
    "v_mfma_f32_16x16x32_bf16 v[4:7], v[232:235], a[180:183], v[4:7]\n\t"
    "v_cvt_pk_bf16_f32 v163, v150, v151\n\t"
    "v_mfma_f32_16x16x32_bf16 v[8:11], v[212:215], a[172:175], 0\n\t"
    "ds_write_b128 v195, v[160:163] offset:19200\n\t"
    "v_mfma_f32_16x16x32_bf16 v[8:11], v[224:227], a[176:179], v[8:11]\n\t"
    "s_waitcnt lgkmcnt(5)\n\t"
    "buffer_store_dwordx2 a[170:171], v201, s[52:55], s86 offen offset:0 sc1\n\t"
    "v_mfma_f32_16x16x32_bf16 v[8:11], v[236:239], a[180:183], v[8:11]\n\t"
    "v_fma_f32 v36, v36, s75, v24\n\t"
    "v_fma_f32 v37, v37, s75, v25\n\t"
    "v_mfma_f32_16x16x32_bf16 v[0:3], v[240:243], a[184:187], v[0:3]\n\t"
    "ds_read_b128 a[172:175], v188 offset:3072\n\t"
    "v_fma_f32 v38, v38, s75, v26\n\t"
    "v_mfma_f32_16x16x32_bf16 v[0:3], v[252:255], a[188:191], v[0:3]\n\t"
    "ds_read_b128 a[176:179], v188 offset:3136\n\t"
    "v_fma_f32 v39, v39, s75, v27\n\t"
    "v_mfma_f32_16x16x32_bf16 v[0:3], v[128:131], a[192:195], v[0:3]\n\t"
    "ds_read_b128 a[180:183], v188 offset:3200\n\t"
    "v_cvt_pk_bf16_f32 v36, v36, v37\n\t"
    "v_mfma_f32_16x16x32_bf16 v[4:7], v[244:247], a[184:187], v[4:7]\n\t"
    "v_cvt_pk_bf16_f32 v37, v38, v39\n\t"
    "v_fma_f32 v40, v40, s75, v28\n\t"
    "v_mfma_f32_16x16x32_bf16 v[4:7], v[120:123], a[188:191], v[4:7]\n\t"
    "v_fma_f32 v41, v41, s75, v29\n\t"
    "v_fma_f32 v42, v42, s75, v30\n\t"
    "v_mfma_f32_16x16x32_bf16 v[4:7], v[132:135], a[192:195], v[4:7]\n\t"
    "v_fma_f32 v43, v43, s75, v31\n\t"
    "v_cvt_pk_bf16_f32 v38, v40, v41\n\t"
    "v_mfma_f32_16x16x32_bf16 v[8:11], v[248:251], a[184:187], v[8:11]\n\t"
    "ds_read_b128 a[184:187], v189 offset:3072\n\t"
    "v_cvt_pk_bf16_f32 v39, v42, v43\n\t"
    "v_mfma_f32_16x16x32_bf16 v[8:11], v[124:127], a[188:191], v[8:11]\n\t"
    "buffer_store_dwordx4 v[36:39], v200, s[52:55], s77 offen offset:0 sc1\n\t"
    "v_mfma_f32_16x16x32_bf16 v[8:11], v[136:139], a[192:195], v[8:11]\n\t"
    "ds_read_b128 a[188:191], v189 offset:3136\n\t"
    "v_fma_f32 v44, v44, s75, v32\n\t"
    "s_waitcnt lgkmcnt(6)\n\t"
    "v_mfma_f32_16x16x32_bf16 v[0:3], v[140:143], a[196:199], v[0:3]\n\t"
    "ds_read_b128 a[192:195], v189 offset:3200\n\t"
    "v_fma_f32 v45, v45, s75, v33\n\t"
    "v_mfma_f32_16x16x32_bf16 v[0:3], v[172:175], a[200:203], v[0:3]\n\t"
    "v_fma_f32 v46, v46, s75, v34\n\t"
    "v_fma_f32 v47, v47, s75, v35\n\t"
    "v_mfma_f32_16x16x32_bf16 v[0:3], v[184:187], a[204:207], v[0:3]\n\t"
    "v_cvt_pk_bf16_f32 v44, v44, v45\n\t"
    "v_cvt_pk_bf16_f32 v45, v46, v47\n\t"
    "v_mfma_f32_16x16x32_bf16 v[4:7], v[164:167], a[196:199], v[4:7]\n\t"
    "ds_write_b64 v210, v[44:45] offset:10240\n\t"
    "v_mfma_f32_16x16x32_bf16 v[4:7], v[176:179], a[200:203], v[4:7]\n\t"
    "v_sub_f32 v144, v56, v72\n\t"
    "v_sub_f32 v145, v57, v73\n\t"
    "v_mfma_f32_16x16x32_bf16 v[4:7], v[48:51], a[204:207], v[4:7]\n\t"
    "ds_write_b128 v196, v[0:3] offset:24576\n\t"
    "v_mfma_f32_16x16x32_bf16 v[8:11], v[168:171], a[196:199], v[8:11]\n\t"
    "v_sub_f32 v146, v58, v74\n\t"
    "v_sub_f32 v147, v59, v75\n\t"
    "s_waitcnt vmcnt(14)\n\t"
    "v_mfma_f32_16x16x32_bf16 v[72:75], a[164:167], a[120:123], 0\n\t"
    "v_cvt_pk_bf16_f32 v152, v144, v145\n\t"
    "v_cvt_pk_bf16_f32 v153, v146, v147\n\t"
    "v_mfma_f32_16x16x32_bf16 v[72:75], a[140:143], a[220:223], v[72:75]\n\t"
    "ds_write_b128 v196, v[4:7] offset:25600\n\t"
    "v_mfma_f32_16x16x32_bf16 v[72:75], a[144:147], a[224:227], v[72:75]\n\t"
    "v_sub_f32 v148, v60, v76\n\t"
    "v_sub_f32 v149, v61, v77\n\t"
    "v_mfma_f32_16x16x32_bf16 v[72:75], a[148:151], a[228:231], v[72:75]\n\t"
    "v_sub_f32 v150, v62, v78\n\t"
    "v_sub_f32 v151, v63, v79\n\t"
    "v_mfma_f32_16x16x32_bf16 v[100:103], a[166:169], a[132:135], 0\n\t"
    "buffer_load_dwordx4 a[132:135], v199, s[40:43], s79 offen offset:64\n\t"
    "s_add_u32 s80, s73, 0x2d000\n\t"
    "v_mfma_f32_16x16x32_bf16 v[100:103], a[152:155], a[208:211], v[100:103]\n\t"
    "ds_read_b128 a[196:199], v190 offset:3072\n\t"
    "v_cvt_pk_bf16_f32 v154, v148, v149\n\t"
    "v_mfma_f32_16x16x32_bf16 v[100:103], a[156:159], a[212:215], v[100:103]\n\t"
    "ds_read_b128 a[140:143], v202 offset:12288\n\t"
    "v_cvt_pk_bf16_f32 v155, v150, v151\n\t"
    "v_mfma_f32_16x16x32_bf16 v[100:103], a[160:163], a[216:219], v[100:103]\n\t"
    "ds_write_b128 v195, v[152:155] offset:28800\n\t"
    "v_mfma_f32_16x16x32_bf16 v[76:79], a[166:169], a[120:123], 0\n\t"
    "v_sub_f32 v144, v104, v96\n\t"
    "v_sub_f32 v145, v105, v97\n\t"
    "v_mfma_f32_16x16x32_bf16 v[76:79], a[152:155], a[220:223], v[76:79]\n\t"
    "v_sub_f32 v146, v106, v98\n\t"
    "v_sub_f32 v147, v107, v99\n\t"
    "v_mfma_f32_16x16x32_bf16 v[76:79], a[156:159], a[224:227], v[76:79]\n\t"
    "ds_read_b128 a[144:147], v202 offset:13312\n\t"
    "v_cvt_pk_bf16_f32 v156, v144, v145\n\t"
    "v_mfma_f32_16x16x32_bf16 v[76:79], a[160:163], a[228:231], v[76:79]\n\t"
    "ds_read_b128 a[148:151], v202 offset:14336\n\t"
    "v_cvt_pk_bf16_f32 v157, v146, v147\n\t"
    "v_mfma_f32_16x16x32_bf16 v[104:107], a[164:167], a[136:139], 0\n\t"
    "ds_read_b128 a[152:155], v202 offset:15360\n\t"
    "v_sub_f32 v148, v108, v100\n\t"
    "v_mfma_f32_16x16x32_bf16 v[8:11], v[180:183], a[200:203], v[8:11]\n\t"
    "ds_read_b128 a[200:203], v190 offset:3136\n\t"
    "v_sub_f32 v149, v109, v101\n\t"
    "v_mfma_f32_16x16x32_bf16 v[8:11], v[52:55], a[204:207], v[8:11]\n\t"
    "ds_read_b128 a[204:207], v190 offset:3200\n\t"
    "v_sub_f32 v150, v110, v102\n\t"
    "s_waitcnt lgkmcnt(14)\n\t"
    "v_mfma_f32_16x16x32_bf16 v[12:15], a[108:111], a[172:175], 0\n\t"
    "ds_read_b128 a[156:159], v202 offset:16384\n\t"
    "v_sub_f32 v151, v111, v103\n\t"
    "v_mfma_f32_16x16x32_bf16 v[12:15], v[216:219], a[176:179], v[12:15]\n\t"
    "ds_read_b128 a[160:163], v202 offset:17408\n\t"
    "v_cvt_pk_bf16_f32 v158, v148, v149\n\t"
    "v_mfma_f32_16x16x32_bf16 v[12:15], v[228:231], a[180:183], v[12:15]\n\t"
    "ds_write_b128 v196, v[8:11] offset:26624\n\t"
    "v_mfma_f32_16x16x32_bf16 v[16:19], a[112:115], a[172:175], 0\n\t"
    "v_cvt_pk_bf16_f32 v159, v150, v151\n\t"
    "v_mfma_f32_16x16x32_bf16 v[16:19], v[220:223], a[176:179], v[16:19]\n\t"
    "v_mfma_f32_16x16x32_bf16 v[16:19], v[232:235], a[180:183], v[16:19]\n\t"
    "ds_write_b128 v195, v[156:159] offset:64\n\t"
    "v_mfma_f32_16x16x32_bf16 v[20:23], v[212:215], a[172:175], 0\n\t"
    "v_add_f32 v144, v88, v96\n\t"
    "v_add_f32 v145, v89, v97\n\t"
    "s_waitcnt lgkmcnt(7)\n\t"
    "v_mfma_f32_16x16x32_bf16 v[104:107], a[140:143], a[208:211], v[104:107]\n\t"
    "v_add_f32 v146, v90, v98\n\t"
    "v_add_f32 v147, v91, v99\n\t"
    "v_mfma_f32_16x16x32_bf16 v[104:107], a[144:147], a[212:215], v[104:107]\n\t"
    "ds_read_b128 a[172:175], v188 offset:6144\n\t"
    "v_cvt_pk_bf16_f32 v160, v144, v145\n\t"
    "v_mfma_f32_16x16x32_bf16 v[104:107], a[148:151], a[216:219], v[104:107]\n\t"
    "v_cvt_pk_bf16_f32 v161, v146, v147\n\t"
    "v_add_f32 v148, v92, v100\n\t"
    "v_mfma_f32_16x16x32_bf16 v[20:23], v[224:227], a[176:179], v[20:23]\n\t"
    "ds_read_b128 a[176:179], v188 offset:6208\n\t"
    "v_add_f32 v149, v93, v101\n\t"
    "v_mfma_f32_16x16x32_bf16 v[20:23], v[236:239], a[180:183], v[20:23]\n\t"
    "ds_read_b128 a[180:183], v188 offset:6272\n\t"
    "v_add_f32 v150, v94, v102\n\t"
    "v_mfma_f32_16x16x32_bf16 v[12:15], v[240:243], a[184:187], v[12:15]\n\t"
    "v_add_f32 v151, v95, v103\n\t"
    "v_cvt_pk_bf16_f32 v162, v148, v149\n\t"
    "v_mfma_f32_16x16x32_bf16 v[12:15], v[252:255], a[188:191], v[12:15]\n\t"
    "v_cvt_pk_bf16_f32 v163, v150, v151\n\t"
    "v_mfma_f32_16x16x32_bf16 v[12:15], v[128:131], a[192:195], v[12:15]\n\t"
    "ds_write_b128 v195, v[160:163] offset:9664\n\t"
    "v_mfma_f32_16x16x32_bf16 v[16:19], v[244:247], a[184:187], v[16:19]\n\t"
    "v_sub_f32 v144, v88, v96\n\t"
    "v_sub_f32 v145, v89, v97\n\t"
    "v_mfma_f32_16x16x32_bf16 v[16:19], v[120:123], a[188:191], v[16:19]\n\t"
    "v_sub_f32 v146, v90, v98\n\t"
    "v_sub_f32 v147, v91, v99\n\t"
    "v_mfma_f32_16x16x32_bf16 v[16:19], v[132:135], a[192:195], v[16:19]\n\t"
    "v_cvt_pk_bf16_f32 v152, v144, v145\n\t"
    "v_cvt_pk_bf16_f32 v153, v146, v147\n\t"
    "v_mfma_f32_16x16x32_bf16 v[20:23], v[248:251], a[184:187], v[20:23]\n\t"
    "ds_read_b128 a[184:187], v189 offset:6144\n\t"
    "v_sub_f32 v148, v92, v100\n\t"
    "v_mfma_f32_16x16x32_bf16 v[20:23], v[124:127], a[188:191], v[20:23]\n\t"
    "ds_read_b128 a[188:191], v189 offset:6208\n\t"
    "v_sub_f32 v149, v93, v101\n\t"
    "v_mfma_f32_16x16x32_bf16 v[20:23], v[136:139], a[192:195], v[20:23]\n\t"
    "ds_read_b128 a[192:195], v189 offset:6272\n\t"
    "v_sub_f32 v150, v94, v102\n\t"
    "s_waitcnt lgkmcnt(11)\n\t"
    "v_mfma_f32_16x16x32_bf16 v[12:15], v[140:143], a[196:199], v[12:15]\n\t"
    "v_sub_f32 v151, v95, v103\n\t"
    "v_cvt_pk_bf16_f32 v154, v148, v149\n\t"
    "v_mfma_f32_16x16x32_bf16 v[12:15], v[172:175], a[200:203], v[12:15]\n\t"
    "v_cvt_pk_bf16_f32 v155, v150, v151\n\t"
    "v_mfma_f32_16x16x32_bf16 v[12:15], v[184:187], a[204:207], v[12:15]\n\t"
    "ds_write_b128 v195, v[152:155] offset:19264\n\t"
    "v_mfma_f32_16x16x32_bf16 v[16:19], v[164:167], a[196:199], v[16:19]\n\t"
    "v_sub_f32 v144, v72, v88\n\t"
    "v_sub_f32 v145, v73, v89\n\t"
    "v_mfma_f32_16x16x32_bf16 v[16:19], v[176:179], a[200:203], v[16:19]\n\t"
    "v_sub_f32 v146, v74, v90\n\t"
    "v_sub_f32 v147, v75, v91\n\t"
    "s_waitcnt vmcnt(14)\n\t"
    "v_mfma_f32_16x16x32_bf16 v[88:91], a[164:167], a[124:127], 0\n\t"
    "ds_write_b128 v196, v[12:15] offset:36864\n\t"
    "v_mfma_f32_16x16x32_bf16 v[88:91], a[140:143], a[220:223], v[88:91]\n\t"
    "v_cvt_pk_bf16_f32 v156, v144, v145\n\t"
    "v_cvt_pk_bf16_f32 v157, v146, v147\n\t"
    "v_mfma_f32_16x16x32_bf16 v[88:91], a[144:147], a[224:227], v[88:91]\n\t"
    "v_sub_f32 v148, v76, v92\n\t"
    "v_sub_f32 v149, v77, v93\n\t"
    "v_mfma_f32_16x16x32_bf16 v[88:91], a[148:151], a[228:231], v[88:91]\n\t"
    "ds_read_b128 a[140:143], v202 offset:0\n\t"
    "v_sub_f32 v150, v78, v94\n\t"
    "v_mfma_f32_16x16x32_bf16 v[108:111], a[166:169], a[136:139], 0\n\t"
    "buffer_load_dwordx4 a[136:139], v199, s[40:43], s80 offen offset:128\n\t"
    "s_add_u32 s81, s70, 0xc6000\n\t"
    "s_waitcnt lgkmcnt(12)\n\t"
    "v_mfma_f32_16x16x32_bf16 v[108:111], a[152:155], a[208:211], v[108:111]\n\t"
    "buffer_load_dwordx4 a[208:211], v198, s[36:39], s81 offen offset:0\n\t"
    "v_mfma_f32_16x16x32_bf16 v[108:111], a[156:159], a[212:215], v[108:111]\n\t"
    "buffer_load_dwordx4 a[212:215], v198, s[36:39], s81 offen offset:64\n\t"
    "v_mfma_f32_16x16x32_bf16 v[108:111], a[160:163], a[216:219], v[108:111]\n\t"
    "buffer_load_dwordx4 a[216:219], v198, s[36:39], s81 offen offset:128\n\t"
    "v_mfma_f32_16x16x32_bf16 v[16:19], v[48:51], a[204:207], v[16:19]\n\t"
    "ds_read_b128 a[144:147], v202 offset:1024\n\t"
    "v_sub_f32 v151, v79, v95\n\t"
    "v_mfma_f32_16x16x32_bf16 v[92:95], a[166:169], a[124:127], 0\n\t"
    "ds_read_b128 a[148:151], v202 offset:2048\n\t"
    "v_cvt_pk_bf16_f32 v158, v148, v149\n\t"
    "v_mfma_f32_16x16x32_bf16 v[92:95], a[152:155], a[220:223], v[92:95]\n\t"
    "buffer_load_dwordx4 a[220:223], v198, s[36:39], s69 offen offset:0\n\t"
    "v_mfma_f32_16x16x32_bf16 v[92:95], a[156:159], a[224:227], v[92:95]\n\t"
    "ds_write_b128 v196, v[16:19] offset:37888\n\t"
    "v_mfma_f32_16x16x32_bf16 v[92:95], a[160:163], a[228:231], v[92:95]\n\t"
    "buffer_load_dwordx4 a[224:227], v198, s[36:39], s69 offen offset:64\n\t"
    "v_mfma_f32_16x16x32_bf16 v[20:23], v[168:171], a[196:199], v[20:23]\n\t"
    "buffer_load_dwordx4 a[228:231], v198, s[36:39], s69 offen offset:128\n\t"
    "v_mfma_f32_16x16x32_bf16 v[20:23], v[180:183], a[200:203], v[20:23]\n\t"
    "ds_read_b128 a[196:199], v190 offset:6144\n\t"
    "v_cvt_pk_bf16_f32 v159, v150, v151\n\t"
    "v_mfma_f32_16x16x32_bf16 v[20:23], v[52:55], a[204:207], v[20:23]\n\t"
    "ds_write_b128 v195, v[156:159] offset:28864\n\t"
    "s_waitcnt lgkmcnt(12)\n\t"
    "v_mfma_f32_16x16x32_bf16 v[24:27], a[108:111], a[172:175], 0\n\t"
    "v_sub_f32 v144, v64, v104\n\t"
    "v_sub_f32 v145, v65, v105\n\t"
    "v_mfma_f32_16x16x32_bf16 v[24:27], v[216:219], a[176:179], v[24:27]\n\t"
    "v_sub_f32 v146, v66, v106\n\t"
    "v_sub_f32 v147, v67, v107\n\t"
    "v_mfma_f32_16x16x32_bf16 v[64:67], a[164:167], a[116:119], 0\n\t"
    "ds_write_b128 v196, v[20:23] offset:38912\n\t"
    "s_waitcnt vmcnt(18) lgkmcnt(4)\n\t"
    "v_mfma_f32_16x16x32_bf16 v[64:67], a[140:143], a[232:235], v[64:67]\n\t"
    "v_cvt_pk_bf16_f32 v160, v144, v145\n\t"
    "v_cvt_pk_bf16_f32 v161, v146, v147\n\t"
    "v_mfma_f32_16x16x32_bf16 v[64:67], a[144:147], a[236:239], v[64:67]\n\t"
    "v_sub_f32 v148, v68, v108\n\t"
    "v_sub_f32 v149, v69, v109\n\t"
    "v_mfma_f32_16x16x32_bf16 v[64:67], a[148:151], a[240:243], v[64:67]\n\t"
    "ds_read_b128 a[200:203], v190 offset:6208\n\t"
    "v_sub_f32 v150, v70, v110\n\t"
    "v_mfma_f32_16x16x32_bf16 v[24:27], v[228:231], a[180:183], v[24:27]\n\t"
    "ds_read_b128 a[204:207], v190 offset:6272\n\t"
    "v_sub_f32 v151, v71, v111\n\t"
    "v_mfma_f32_16x16x32_bf16 v[28:31], a[112:115], a[172:175], 0\n\t"
    "ds_read_b128 a[152:155], v202 offset:3072\n\t"
    "v_cvt_pk_bf16_f32 v162, v148, v149\n\t"
    "v_mfma_f32_16x16x32_bf16 v[28:31], v[220:223], a[176:179], v[28:31]\n\t"
    "ds_read_b128 a[156:159], v202 offset:4096\n\t"
    "v_cvt_pk_bf16_f32 v163, v150, v151\n\t"
    "v_mfma_f32_16x16x32_bf16 v[28:31], v[232:235], a[180:183], v[28:31]\n\t"
    "ds_write_b128 v195, v[160:163] offset:128\n\t"
    "v_mfma_f32_16x16x32_bf16 v[32:35], v[212:215], a[172:175], 0\n\t"
    "v_add_f32 v144, v112, v104\n\t"
    "v_add_f32 v145, v113, v105\n\t"
    "v_mfma_f32_16x16x32_bf16 v[32:35], v[224:227], a[176:179], v[32:35]\n\t"
    "v_add_f32 v146, v114, v106\n\t"
    "v_add_f32 v147, v115, v107\n\t"
    "v_mfma_f32_16x16x32_bf16 v[32:35], v[236:239], a[180:183], v[32:35]\n\t"
    "ds_read_b128 a[172:175], v191 offset:0\n\t"
    "v_cvt_pk_bf16_f32 v152, v144, v145\n\t"
    "v_mfma_f32_16x16x32_bf16 v[24:27], v[240:243], a[184:187], v[24:27]\n\t"
    "ds_read_b128 a[176:179], v191 offset:64\n\t"
    "v_cvt_pk_bf16_f32 v153, v146, v147\n\t"
    "v_mfma_f32_16x16x32_bf16 v[24:27], v[252:255], a[188:191], v[24:27]\n\t"
    "ds_read_b128 a[180:183], v191 offset:128\n\t"
    "v_add_f32 v148, v116, v108\n\t"
    "v_mfma_f32_16x16x32_bf16 v[24:27], v[128:131], a[192:195], v[24:27]\n\t"
    "ds_read_b128 a[160:163], v202 offset:5120\n\t"
    "v_add_f32 v149, v117, v109\n\t"
    "v_mfma_f32_16x16x32_bf16 v[28:31], v[244:247], a[184:187], v[28:31]\n\t"
    "v_add_f32 v150, v118, v110\n\t"
    "v_add_f32 v151, v119, v111\n\t"
    "v_mfma_f32_16x16x32_bf16 v[28:31], v[120:123], a[188:191], v[28:31]\n\t"
    "v_cvt_pk_bf16_f32 v154, v148, v149\n\t"
    "v_cvt_pk_bf16_f32 v155, v150, v151\n\t"
    "v_mfma_f32_16x16x32_bf16 v[28:31], v[132:135], a[192:195], v[28:31]\n\t"
    "ds_write_b128 v195, v[152:155] offset:9728\n\t"
    "v_mfma_f32_16x16x32_bf16 v[32:35], v[248:251], a[184:187], v[32:35]\n\t"
    "v_sub_f32 v144, v112, v104\n\t"
    "v_sub_f32 v145, v113, v105\n\t"
    "v_mfma_f32_16x16x32_bf16 v[32:35], v[124:127], a[188:191], v[32:35]\n\t"
    "v_sub_f32 v146, v114, v106\n\t"
    "v_sub_f32 v147, v115, v107\n\t"
    "v_mfma_f32_16x16x32_bf16 v[32:35], v[136:139], a[192:195], v[32:35]\n\t"
    "ds_read_b128 a[184:187], v192 offset:0\n\t"
    "v_cvt_pk_bf16_f32 v156, v144, v145\n\t"
    "s_waitcnt lgkmcnt(9)\n\t"
    "v_mfma_f32_16x16x32_bf16 v[24:27], v[140:143], a[196:199], v[24:27]\n\t"
    "ds_read_b128 a[188:191], v192 offset:64\n\t"
    "v_cvt_pk_bf16_f32 v157, v146, v147\n\t"
    "v_mfma_f32_16x16x32_bf16 v[24:27], v[172:175], a[200:203], v[24:27]\n\t"
    "ds_read_b128 a[192:195], v192 offset:128\n\t"
    "v_sub_f32 v148, v116, v108\n\t"
    "v_mfma_f32_16x16x32_bf16 v[24:27], v[184:187], a[204:207], v[24:27]\n\t"
    "v_sub_f32 v149, v117, v109\n\t"
    "v_sub_f32 v150, v118, v110\n\t"
    "v_mfma_f32_16x16x32_bf16 v[28:31], v[164:167], a[196:199], v[28:31]\n\t"
    "v_sub_f32 v151, v119, v111\n\t"
    "v_cvt_pk_bf16_f32 v158, v148, v149\n\t"
    "v_mfma_f32_16x16x32_bf16 v[28:31], v[176:179], a[200:203], v[28:31]\n\t"
    "v_cvt_pk_bf16_f32 v159, v150, v151\n\t"
    "v_mfma_f32_16x16x32_bf16 v[28:31], v[48:51], a[204:207], v[28:31]\n\t"
    "ds_write_b128 v196, v[24:27] offset:0\n\t"
    "v_mfma_f32_16x16x32_bf16 v[32:35], v[168:171], a[196:199], v[32:35]\n\t"
    "v_mfma_f32_16x16x32_bf16 v[32:35], v[180:183], a[200:203], v[32:35]\n\t"
    "v_mfma_f32_16x16x32_bf16 v[32:35], v[52:55], a[204:207], v[32:35]\n\t"
    "ds_write_b128 v195, v[156:159] offset:19328\n\t"
    "s_waitcnt lgkmcnt(7)\n\t"
    "v_mfma_f32_16x16x32_bf16 v[0:3], a[0:3], a[172:175], v[0:3]\n\t"
    "v_sub_f32 v144, v88, v112\n\t"
    "v_sub_f32 v145, v89, v113\n\t"
    "v_mfma_f32_16x16x32_bf16 v[0:3], a[12:15], a[176:179], v[0:3]\n\t"
    "v_sub_f32 v146, v90, v114\n\t"
    "v_sub_f32 v147, v91, v115\n\t"
    "s_waitcnt vmcnt(14)\n\t"
    "v_mfma_f32_16x16x32_bf16 v[112:115], a[164:167], a[128:131], 0\n\t"
    "ds_write_b128 v196, v[28:31] offset:1024\n\t"
    "v_mfma_f32_16x16x32_bf16 v[112:115], a[140:143], a[244:247], v[112:115]\n\t"
    "v_cvt_pk_bf16_f32 v160, v144, v145\n\t"
    "v_cvt_pk_bf16_f32 v161, v146, v147\n\t"
    "v_mfma_f32_16x16x32_bf16 v[112:115], a[144:147], a[248:251], v[112:115]\n\t"
    "v_sub_f32 v148, v92, v116\n\t"
    "v_sub_f32 v149, v93, v117\n\t"
    "v_mfma_f32_16x16x32_bf16 v[112:115], a[148:151], a[252:255], v[112:115]\n\t"
    "ds_write_b128 v196, v[32:35] offset:2048\n\t"
    "v_mfma_f32_16x16x32_bf16 v[68:71], a[166:169], a[116:119], 0\n\t"
    "buffer_load_dwordx4 a[116:119], v199, s[40:43], s72 offen offset:0\n\t"
    "s_waitcnt lgkmcnt(8)\n\t"
    "v_mfma_f32_16x16x32_bf16 v[68:71], a[152:155], a[232:235], v[68:71]\n\t"
    "v_sub_f32 v150, v94, v118\n\t"
    "v_sub_f32 v151, v95, v119\n\t"
    "v_mfma_f32_16x16x32_bf16 v[68:71], a[156:159], a[236:239], v[68:71]\n\t"
    "ds_read_b128 a[196:199], v193 offset:0\n\t"
    "v_cvt_pk_bf16_f32 v162, v148, v149\n\t"
    "v_mfma_f32_16x16x32_bf16 v[68:71], a[160:163], a[240:243], v[68:71]\n\t"
    "ds_read_b128 a[200:203], v193 offset:64\n\t"
    "v_cvt_pk_bf16_f32 v163, v150, v151\n\t"
    "v_mfma_f32_16x16x32_bf16 v[116:119], a[166:169], a[128:131], 0\n\t"
    "ds_write_b128 v195, v[160:163] offset:28928\n\t"
    "v_mfma_f32_16x16x32_bf16 v[116:119], a[152:155], a[244:247], v[116:119]\n\t"
    "v_sub_f32 v144, v80, v64\n\t"
    "v_sub_f32 v145, v81, v65\n\t"
    "v_mfma_f32_16x16x32_bf16 v[116:119], a[156:159], a[248:251], v[116:119]\n\t"
    "v_sub_f32 v146, v82, v66\n\t"
    "v_sub_f32 v147, v83, v67\n\t"
    "v_mfma_f32_16x16x32_bf16 v[116:119], a[160:163], a[252:255], v[116:119]\n\t"
    "ds_read_b128 a[204:207], v193 offset:128\n\t"
    "v_cvt_pk_bf16_f32 v152, v144, v145\n\t"
    "v_mfma_f32_16x16x32_bf16 v[80:83], a[164:167], a[120:123], 0\n\t"
    "ds_read_b128 a[140:143], v202 offset:6144\n\t"
    "v_cvt_pk_bf16_f32 v153, v146, v147\n\t"
    "v_mfma_f32_16x16x32_bf16 v[0:3], a[24:27], a[180:183], v[0:3]\n\t"
    "ds_read_b128 a[144:147], v202 offset:7168\n\t"
    "v_sub_f32 v148, v84, v68\n\t"
    "v_mfma_f32_16x16x32_bf16 v[4:7], a[4:7], a[172:175], v[4:7]\n\t"
    "ds_read_b128 a[148:151], v202 offset:8192\n\t"
    "v_sub_f32 v149, v85, v69\n\t"
    "v_mfma_f32_16x16x32_bf16 v[4:7], a[16:19], a[176:179], v[4:7]\n\t"
    "ds_read_b128 a[152:155], v202 offset:9216\n\t"
    "v_sub_f32 v150, v86, v70\n\t"
    "v_mfma_f32_16x16x32_bf16 v[4:7], a[28:31], a[180:183], v[4:7]\n\t"
    "ds_read_b128 a[156:159], v202 offset:10240\n\t"
    "v_sub_f32 v151, v87, v71\n\t"
    "v_mfma_f32_16x16x32_bf16 v[8:11], a[8:11], a[172:175], v[8:11]\n\t"
    "ds_read_b128 a[172:175], v191 offset:3072\n\t"
    "v_cvt_pk_bf16_f32 v154, v148, v149\n\t"
    "v_mfma_f32_16x16x32_bf16 v[8:11], a[20:23], a[176:179], v[8:11]\n\t"
    "ds_read_b128 a[176:179], v191 offset:3136\n\t"
    "v_cvt_pk_bf16_f32 v155, v150, v151\n\t"
    "v_mfma_f32_16x16x32_bf16 v[8:11], a[32:35], a[180:183], v[8:11]\n\t"
    "ds_read_b128 a[180:183], v191 offset:3200\n\t"
    "s_waitcnt lgkmcnt(12)\n\t"
    "s_barrier\n\t"
    "v_mfma_f32_16x16x32_bf16 v[0:3], a[36:39], a[184:187], v[0:3]\n\t"
    "ds_read_b128 v[36:39], v197 offset:24576\n\t"
    "v_mfma_f32_16x16x32_bf16 v[0:3], a[48:51], a[188:191], v[0:3]\n\t"
    "ds_read_b128 v[40:43], v197 offset:25600\n\t"
    "v_mfma_f32_16x16x32_bf16 v[0:3], a[60:63], a[192:195], v[0:3]\n\t"
    "ds_read_b128 v[44:47], v197 offset:26624\n\t"
    "s_add_u32 s76, s74, 0x48000\n\t"
    "v_mfma_f32_16x16x32_bf16 v[4:7], a[40:43], a[184:187], v[4:7]\n\t"
    "ds_read_b64 a[170:171], v211 offset:8192\n\t"
    "s_add_u32 s86, s74, 0x24c00\n\t"
    "v_mfma_f32_16x16x32_bf16 v[4:7], a[52:55], a[188:191], v[4:7]\n\t"
    "ds_read_b128 a[160:163], v202 offset:11264\n\t"
    "v_mfma_f32_16x16x32_bf16 v[4:7], a[64:67], a[192:195], v[4:7]\n\t"
    "s_waitcnt lgkmcnt(10)\n\t"
    "v_mfma_f32_16x16x32_bf16 v[80:83], a[140:143], a[232:235], v[80:83]\n\t"
    "v_mfma_f32_16x16x32_bf16 v[80:83], a[144:147], a[236:239], v[80:83]\n\t"
    "v_mfma_f32_16x16x32_bf16 v[80:83], a[148:151], a[240:243], v[80:83]\n\t"
    "v_mfma_f32_16x16x32_bf16 v[8:11], a[44:47], a[184:187], v[8:11]\n\t"
    "ds_read_b128 a[184:187], v192 offset:3072\n\t"
    "v_mfma_f32_16x16x32_bf16 v[8:11], a[56:59], a[188:191], v[8:11]\n\t"
    "ds_read_b128 a[188:191], v192 offset:3136\n\t"
    "v_mfma_f32_16x16x32_bf16 v[8:11], a[68:71], a[192:195], v[8:11]\n\t"
    "ds_read_b128 a[192:195], v192 offset:3200\n\t"
    "v_mfma_f32_16x16x32_bf16 v[0:3], a[72:75], a[196:199], v[0:3]\n\t"
    "v_mfma_f32_16x16x32_bf16 v[0:3], a[84:87], a[200:203], v[0:3]\n\t"
    "v_mfma_f32_16x16x32_bf16 v[0:3], a[96:99], a[204:207], v[0:3]\n\t"
    "v_mfma_f32_16x16x32_bf16 v[4:7], a[76:79], a[196:199], v[4:7]\n\t"
    "s_waitcnt lgkmcnt(4)\n\t"
    "buffer_store_dwordx2 a[170:171], v201, s[52:55], s86 offen offset:0 sc1\n\t"
    "v_mfma_f32_16x16x32_bf16 v[4:7], a[88:91], a[200:203], v[4:7]\n\t"
    "ds_read_b64 a[170:171], v211 offset:10240\n\t"
    "s_add_u32 s86, s74, 0x25800\n\t"
    "v_mfma_f32_16x16x32_bf16 v[4:7], a[100:103], a[204:207], v[4:7]\n\t"
    "v_mfma_f32_16x16x32_bf16 v[8:11], a[80:83], a[196:199], v[8:11]\n\t"
    "ds_read_b128 a[196:199], v193 offset:3072\n\t"
    "v_fma_f32 v36, v36, s75, v0\n\t"
    "v_mfma_f32_16x16x32_bf16 v[8:11], a[92:95], a[200:203], v[8:11]\n\t"
    "ds_read_b128 a[200:203], v193 offset:3136\n\t"
    "v_fma_f32 v37, v37, s75, v1\n\t"
    "v_mfma_f32_16x16x32_bf16 v[8:11], a[104:107], a[204:207], v[8:11]\n\t"
    "ds_read_b128 a[204:207], v193 offset:3200\n\t"
    "v_fma_f32 v38, v38, s75, v2\n\t"
    "v_mfma_f32_16x16x32_bf16 v[12:15], a[0:3], a[172:175], v[12:15]\n\t"
    "v_fma_f32 v39, v39, s75, v3\n\t"
    "v_cvt_pk_bf16_f32 v36, v36, v37\n\t"
    "v_mfma_f32_16x16x32_bf16 v[12:15], a[12:15], a[176:179], v[12:15]\n\t"
    "v_cvt_pk_bf16_f32 v37, v38, v39\n\t"
    "v_fma_f32 v40, v40, s75, v4\n\t"
    "v_mfma_f32_16x16x32_bf16 v[12:15], a[24:27], a[180:183], v[12:15]\n\t"
    "v_fma_f32 v41, v41, s75, v5\n\t"
    "v_fma_f32 v42, v42, s75, v6\n\t"
    "v_mfma_f32_16x16x32_bf16 v[16:19], a[4:7], a[172:175], v[16:19]\n\t"
    "v_fma_f32 v43, v43, s75, v7\n\t"
    "v_cvt_pk_bf16_f32 v38, v40, v41\n\t"
    "v_mfma_f32_16x16x32_bf16 v[16:19], a[16:19], a[176:179], v[16:19]\n\t"
    "v_cvt_pk_bf16_f32 v39, v42, v43\n\t"
    "v_mfma_f32_16x16x32_bf16 v[16:19], a[28:31], a[180:183], v[16:19]\n\t"
    "buffer_store_dwordx4 v[36:39], v200, s[52:55], s76 offen offset:0 sc1\n\t"
    "v_mfma_f32_16x16x32_bf16 v[20:23], a[8:11], a[172:175], v[20:23]\n\t"
    "s_waitcnt lgkmcnt(3)\n\t"
    "buffer_store_dwordx2 a[170:171], v201, s[52:55], s86 offen offset:0 sc1\n\t"
    "v_mfma_f32_16x16x32_bf16 v[20:23], a[20:23], a[176:179], v[20:23]\n\t"
    "ds_read_b128 a[172:175], v191 offset:6144\n\t"
    "v_fma_f32 v44, v44, s75, v8\n\t"
    "v_mfma_f32_16x16x32_bf16 v[20:23], a[32:35], a[180:183], v[20:23]\n\t"
    "ds_read_b128 a[176:179], v191 offset:6208\n\t"
    "v_fma_f32 v45, v45, s75, v9\n\t"
    "v_mfma_f32_16x16x32_bf16 v[12:15], a[36:39], a[184:187], v[12:15]\n\t"
    "ds_read_b128 a[180:183], v191 offset:6272\n\t"
    "v_fma_f32 v46, v46, s75, v10\n\t"
    "v_mfma_f32_16x16x32_bf16 v[12:15], a[48:51], a[188:191], v[12:15]\n\t"
    "v_fma_f32 v47, v47, s75, v11\n\t"
    "v_cvt_pk_bf16_f32 v44, v44, v45\n\t"
    "v_mfma_f32_16x16x32_bf16 v[12:15], a[60:63], a[192:195], v[12:15]\n\t"
    "v_cvt_pk_bf16_f32 v45, v46, v47\n\t"
    "v_mfma_f32_16x16x32_bf16 v[16:19], a[40:43], a[184:187], v[16:19]\n\t"
    "ds_write_b64 v210, v[44:45] offset:0\n\t"
    "v_mfma_f32_16x16x32_bf16 v[16:19], a[52:55], a[188:191], v[16:19]\n\t"
    "v_mfma_f32_16x16x32_bf16 v[16:19], a[64:67], a[192:195], v[16:19]\n\t"
    "ds_read_b128 v[36:39], v197 offset:36864\n\t"
    "v_mfma_f32_16x16x32_bf16 v[20:23], a[44:47], a[184:187], v[20:23]\n\t"
    "ds_read_b128 a[184:187], v192 offset:6144\n\t"
    "v_mfma_f32_16x16x32_bf16 v[20:23], a[56:59], a[188:191], v[20:23]\n\t"
    "ds_read_b128 a[188:191], v192 offset:6208\n\t"
    "v_mfma_f32_16x16x32_bf16 v[20:23], a[68:71], a[192:195], v[20:23]\n\t"
    "ds_read_b128 a[192:195], v192 offset:6272\n\t"
    "s_waitcnt lgkmcnt(8)\n\t"
    "v_mfma_f32_16x16x32_bf16 v[12:15], a[72:75], a[196:199], v[12:15]\n\t"
    "ds_read_b128 v[40:43], v197 offset:37888\n\t"
    "v_mfma_f32_16x16x32_bf16 v[12:15], a[84:87], a[200:203], v[12:15]\n\t"
    "ds_read_b128 v[44:47], v197 offset:38912\n\t"
    "s_add_u32 s77, s74, 0x48c00\n\t"
    "v_mfma_f32_16x16x32_bf16 v[12:15], a[96:99], a[204:207], v[12:15]\n\t"
    "v_mfma_f32_16x16x32_bf16 v[16:19], a[76:79], a[196:199], v[16:19]\n\t"
    "v_mfma_f32_16x16x32_bf16 v[16:19], a[88:91], a[200:203], v[16:19]\n\t"
    "v_mfma_f32_16x16x32_bf16 v[16:19], a[100:103], a[204:207], v[16:19]\n\t"
    "v_mfma_f32_16x16x32_bf16 v[20:23], a[80:83], a[196:199], v[20:23]\n\t"
    "ds_read_b128 a[196:199], v193 offset:6144\n\t"
    "v_mfma_f32_16x16x32_bf16 v[20:23], a[92:95], a[200:203], v[20:23]\n\t"
    "ds_read_b128 a[200:203], v193 offset:6208\n\t"
    "v_mfma_f32_16x16x32_bf16 v[20:23], a[104:107], a[204:207], v[20:23]\n\t"
    "ds_read_b128 a[204:207], v193 offset:6272\n\t"
    "s_waitcnt lgkmcnt(10)\n\t"
    "v_mfma_f32_16x16x32_bf16 v[24:27], a[0:3], a[172:175], v[24:27]\n\t"
    "v_mfma_f32_16x16x32_bf16 v[24:27], a[12:15], a[176:179], v[24:27]\n\t"
    "v_mfma_f32_16x16x32_bf16 v[24:27], a[24:27], a[180:183], v[24:27]\n\t"
    "v_mfma_f32_16x16x32_bf16 v[28:31], a[4:7], a[172:175], v[28:31]\n\t"
    "v_mfma_f32_16x16x32_bf16 v[28:31], a[16:19], a[176:179], v[28:31]\n\t"
    "s_waitcnt lgkmcnt(3)\n\t"
    "v_fma_f32 v36, v36, s75, v12\n\t"
    "v_fma_f32 v37, v37, s75, v13\n\t"
    "v_mfma_f32_16x16x32_bf16 v[28:31], a[28:31], a[180:183], v[28:31]\n\t"
    "v_fma_f32 v38, v38, s75, v14\n\t"
    "v_fma_f32 v39, v39, s75, v15\n\t"
    "v_mfma_f32_16x16x32_bf16 v[32:35], a[8:11], a[172:175], v[32:35]\n\t"
    "v_cvt_pk_bf16_f32 v36, v36, v37\n\t"
    "v_cvt_pk_bf16_f32 v37, v38, v39\n\t"
    "v_mfma_f32_16x16x32_bf16 v[32:35], a[20:23], a[176:179], v[32:35]\n\t"
    "v_fma_f32 v40, v40, s75, v16\n\t"
    "v_fma_f32 v41, v41, s75, v17\n\t"
    "v_mfma_f32_16x16x32_bf16 v[32:35], a[32:35], a[180:183], v[32:35]\n\t"
    "v_fma_f32 v42, v42, s75, v18\n\t"
    "v_fma_f32 v43, v43, s75, v19\n\t"
    "v_cvt_pk_bf16_f32 v38, v40, v41\n\t"
    "v_cvt_pk_bf16_f32 v39, v42, v43\n\t"
    "buffer_store_dwordx4 v[36:39], v200, s[52:55], s77 offen offset:0 sc1\n\t"
    "s_waitcnt lgkmcnt(0)\n\t"
    "s_barrier\n\t"
    "v_mfma_f32_16x16x32_bf16 v[24:27], a[36:39], a[184:187], v[24:27]\n\t"
    "ds_write_b128 v194, v[152:155] offset:0\n\t"
    "v_mfma_f32_16x16x32_bf16 v[24:27], a[48:51], a[188:191], v[24:27]\n\t"
    "v_fma_f32 v44, v44, s75, v20\n\t"
    "v_fma_f32 v45, v45, s75, v21\n\t"
    "v_mfma_f32_16x16x32_bf16 v[24:27], a[60:63], a[192:195], v[24:27]\n\t"
    "v_fma_f32 v46, v46, s75, v22\n\t"
    "v_fma_f32 v47, v47, s75, v23\n\t"
    "v_mfma_f32_16x16x32_bf16 v[28:31], a[40:43], a[184:187], v[28:31]\n\t"
    "ds_read_b128 a[172:175], v188 offset:38400\n\t"
    "v_cvt_pk_bf16_f32 v44, v44, v45\n\t"
    "v_mfma_f32_16x16x32_bf16 v[28:31], a[52:55], a[188:191], v[28:31]\n\t"
    "ds_read_b128 a[176:179], v188 offset:38464\n\t"
    "v_cvt_pk_bf16_f32 v45, v46, v47\n\t"
    "v_mfma_f32_16x16x32_bf16 v[28:31], a[64:67], a[192:195], v[28:31]\n\t"
    "ds_write_b64 v210, v[44:45] offset:2048\n\t"
    "v_mfma_f32_16x16x32_bf16 v[32:35], a[44:47], a[184:187], v[32:35]\n\t"
    "v_add_f32 v144, v56, v64\n\t"
    "v_add_f32 v145, v57, v65\n\t"
    "v_mfma_f32_16x16x32_bf16 v[32:35], a[56:59], a[188:191], v[32:35]\n\t"
    "ds_read_b128 a[180:183], v188 offset:38528\n\t"
    "v_add_f32 v146, v58, v66\n\t"
    "v_mfma_f32_16x16x32_bf16 v[32:35], a[68:71], a[192:195], v[32:35]\n\t"
    "ds_read_b128 a[184:187], v189 offset:38400\n\t"
    "v_add_f32 v147, v59, v67\n\t"
    "v_mfma_f32_16x16x32_bf16 v[24:27], a[72:75], a[196:199], v[24:27]\n\t"
    "ds_read_b128 a[188:191], v189 offset:38464\n\t"
    "v_cvt_pk_bf16_f32 v156, v144, v145\n\t"
    "v_mfma_f32_16x16x32_bf16 v[24:27], a[84:87], a[200:203], v[24:27]\n\t"
    "ds_read_b128 a[192:195], v189 offset:38528\n\t"
    "v_cvt_pk_bf16_f32 v157, v146, v147\n\t"
    "v_mfma_f32_16x16x32_bf16 v[24:27], a[96:99], a[204:207], v[24:27]\n\t"
    "ds_read_b128 v[36:39], v197 offset:0\n\t"
    "v_add_f32 v148, v60, v68\n\t"
    "v_mfma_f32_16x16x32_bf16 v[28:31], a[76:79], a[196:199], v[28:31]\n\t"
    "ds_read_b128 v[40:43], v197 offset:1024\n\t"
    "v_add_f32 v149, v61, v69\n\t"
    "v_mfma_f32_16x16x32_bf16 v[28:31], a[88:91], a[200:203], v[28:31]\n\t"
    "ds_read_b128 v[44:47], v197 offset:2048\n\t"
    "s_add_u32 s76, s74, 0x49800\n\t"
    "v_add_f32 v150, v62, v70\n\t"
    "v_mfma_f32_16x16x32_bf16 v[28:31], a[100:103], a[204:207], v[28:31]\n\t"
    "ds_read_b64 a[170:171], v211 offset:0\n\t"
    "s_add_u32 s86, s74, 0x48000\n\t"
    "v_add_f32 v151, v63, v71\n\t"
    "v_mfma_f32_16x16x32_bf16 v[32:35], a[80:83], a[196:199], v[32:35]\n\t"
    "ds_read_b128 a[196:199], v190 offset:38400\n\t"
    "v_cvt_pk_bf16_f32 v158, v148, v149\n\t"
    "v_mfma_f32_16x16x32_bf16 v[32:35], a[92:95], a[200:203], v[32:35]\n\t"
    "ds_read_b128 a[200:203], v190 offset:38464\n\t"
    "v_cvt_pk_bf16_f32 v159, v150, v151\n\t"
    "v_mfma_f32_16x16x32_bf16 v[32:35], a[104:107], a[204:207], v[32:35]\n\t"
    "ds_write_b128 v194, v[156:159] offset:9600\n\t"
    "v_sub_f32 v144, v56, v64\n\t"
    "s_waitcnt lgkmcnt(10)\n\t"
    "v_mfma_f32_16x16x32_bf16 v[0:3], a[108:111], a[172:175], 0\n\t"
    "v_sub_f32 v145, v57, v65\n\t"
    "v_sub_f32 v146, v58, v66\n\t"
    "v_mfma_f32_16x16x32_bf16 v[0:3], v[216:219], a[176:179], v[0:3]\n\t"
    "v_sub_f32 v147, v59, v67\n\t"
    "v_cvt_pk_bf16_f32 v160, v144, v145\n\t"
    "v_mfma_f32_16x16x32_bf16 v[0:3], v[228:231], a[180:183], v[0:3]\n\t"
    "ds_read_b128 a[204:207], v190 offset:38528\n\t"
    "v_cvt_pk_bf16_f32 v161, v146, v147\n\t"
    "v_mfma_f32_16x16x32_bf16 v[4:7], a[112:115], a[172:175], 0\n\t"
    "v_sub_f32 v148, v60, v68\n\t"
    "v_sub_f32 v149, v61, v69\n\t"
    "v_mfma_f32_16x16x32_bf16 v[4:7], v[220:223], a[176:179], v[4:7]\n\t"
    "v_sub_f32 v150, v62, v70\n\t"
    "v_sub_f32 v151, v63, v71\n\t"
    "v_mfma_f32_16x16x32_bf16 v[4:7], v[232:235], a[180:183], v[4:7]\n\t"
    "v_cvt_pk_bf16_f32 v162, v148, v149\n\t"
    "v_cvt_pk_bf16_f32 v163, v150, v151\n\t"
    "v_mfma_f32_16x16x32_bf16 v[8:11], v[212:215], a[172:175], 0\n\t"
    "ds_write_b128 v194, v[160:163] offset:19200\n\t"
    "v_mfma_f32_16x16x32_bf16 v[8:11], v[224:227], a[176:179], v[8:11]\n\t"
    "s_waitcnt lgkmcnt(5)\n\t"
    "buffer_store_dwordx2 a[170:171], v201, s[52:55], s86 offen offset:0 sc1\n\t"
    "v_mfma_f32_16x16x32_bf16 v[8:11], v[236:239], a[180:183], v[8:11]\n\t"
    "v_fma_f32 v36, v36, s75, v24\n\t"
    "v_fma_f32 v37, v37, s75, v25\n\t"
    "v_mfma_f32_16x16x32_bf16 v[0:3], v[240:243], a[184:187], v[0:3]\n\t"
    "ds_read_b128 a[172:175], v188 offset:41472\n\t"
    "v_fma_f32 v38, v38, s75, v26\n\t"
    "v_mfma_f32_16x16x32_bf16 v[0:3], v[252:255], a[188:191], v[0:3]\n\t"
    "ds_read_b128 a[176:179], v188 offset:41536\n\t"
    "v_fma_f32 v39, v39, s75, v27\n\t"
    "v_mfma_f32_16x16x32_bf16 v[0:3], v[128:131], a[192:195], v[0:3]\n\t"
    "ds_read_b128 a[180:183], v188 offset:41600\n\t"
    "v_cvt_pk_bf16_f32 v36, v36, v37\n\t"
    "v_mfma_f32_16x16x32_bf16 v[4:7], v[244:247], a[184:187], v[4:7]\n\t"
    "v_cvt_pk_bf16_f32 v37, v38, v39\n\t"
    "v_fma_f32 v40, v40, s75, v28\n\t"
    "v_mfma_f32_16x16x32_bf16 v[4:7], v[120:123], a[188:191], v[4:7]\n\t"
    "v_fma_f32 v41, v41, s75, v29\n\t"
    "v_fma_f32 v42, v42, s75, v30\n\t"
    "v_mfma_f32_16x16x32_bf16 v[4:7], v[132:135], a[192:195], v[4:7]\n\t"
    "v_fma_f32 v43, v43, s75, v31\n\t"
    "v_cvt_pk_bf16_f32 v38, v40, v41\n\t"
    "v_mfma_f32_16x16x32_bf16 v[8:11], v[248:251], a[184:187], v[8:11]\n\t"
    "ds_read_b128 a[184:187], v189 offset:41472\n\t"
    "v_cvt_pk_bf16_f32 v39, v42, v43\n\t"
    "v_mfma_f32_16x16x32_bf16 v[8:11], v[124:127], a[188:191], v[8:11]\n\t"
    "buffer_store_dwordx4 v[36:39], v200, s[52:55], s76 offen offset:0 sc1\n\t"
    "v_mfma_f32_16x16x32_bf16 v[8:11], v[136:139], a[192:195], v[8:11]\n\t"
    "ds_read_b128 a[188:191], v189 offset:41536\n\t"
    "v_fma_f32 v44, v44, s75, v32\n\t"
    "s_waitcnt lgkmcnt(6)\n\t"
    "v_mfma_f32_16x16x32_bf16 v[0:3], v[140:143], a[196:199], v[0:3]\n\t"
    "ds_read_b128 a[192:195], v189 offset:41600\n\t"
    "v_fma_f32 v45, v45, s75, v33\n\t"
    "v_mfma_f32_16x16x32_bf16 v[0:3], v[172:175], a[200:203], v[0:3]\n\t"
    "v_fma_f32 v46, v46, s75, v34\n\t"
    "v_fma_f32 v47, v47, s75, v35\n\t"
    "v_mfma_f32_16x16x32_bf16 v[0:3], v[184:187], a[204:207], v[0:3]\n\t"
    "v_cvt_pk_bf16_f32 v44, v44, v45\n\t"
    "v_cvt_pk_bf16_f32 v45, v46, v47\n\t"
    "v_mfma_f32_16x16x32_bf16 v[4:7], v[164:167], a[196:199], v[4:7]\n\t"
    "ds_write_b64 v210, v[44:45] offset:4096\n\t"
    "v_mfma_f32_16x16x32_bf16 v[4:7], v[176:179], a[200:203], v[4:7]\n\t"
    "v_sub_f32 v144, v112, v56\n\t"
    "v_sub_f32 v145, v113, v57\n\t"
    "v_mfma_f32_16x16x32_bf16 v[4:7], v[48:51], a[204:207], v[4:7]\n\t"
    "ds_write_b128 v196, v[0:3] offset:12288\n\t"
    "v_mfma_f32_16x16x32_bf16 v[8:11], v[168:171], a[196:199], v[8:11]\n\t"
    "v_sub_f32 v146, v114, v58\n\t"
    "v_sub_f32 v147, v115, v59\n\t"
    "s_waitcnt vmcnt(14)\n\t"
    "v_mfma_f32_16x16x32_bf16 v[56:59], a[164:167], a[132:135], 0\n\t"
    "v_cvt_pk_bf16_f32 v152, v144, v145\n\t"
    "v_cvt_pk_bf16_f32 v153, v146, v147\n\t"
    "v_mfma_f32_16x16x32_bf16 v[56:59], a[140:143], a[244:247], v[56:59]\n\t"
    "ds_write_b128 v196, v[4:7] offset:13312\n\t"
    "v_mfma_f32_16x16x32_bf16 v[56:59], a[144:147], a[248:251], v[56:59]\n\t"
    "v_sub_f32 v148, v116, v60\n\t"
    "v_sub_f32 v149, v117, v61\n\t"
    "v_mfma_f32_16x16x32_bf16 v[56:59], a[148:151], a[252:255], v[56:59]\n\t"
    "v_sub_f32 v150, v118, v62\n\t"
    "v_sub_f32 v151, v119, v63\n\t"
    "v_mfma_f32_16x16x32_bf16 v[84:87], a[166:169], a[120:123], 0\n\t"
    "buffer_load_dwordx4 a[120:123], v199, s[40:43], s72 offen offset:64\n\t"
    "v_mfma_f32_16x16x32_bf16 v[84:87], a[152:155], a[232:235], v[84:87]\n\t"
    "ds_read_b128 a[196:199], v190 offset:41472\n\t"
    "v_cvt_pk_bf16_f32 v154, v148, v149\n\t"
    "v_mfma_f32_16x16x32_bf16 v[84:87], a[156:159], a[236:239], v[84:87]\n\t"
    "ds_read_b128 a[140:143], v202 offset:12288\n\t"
    "v_cvt_pk_bf16_f32 v155, v150, v151\n\t"
    "v_mfma_f32_16x16x32_bf16 v[84:87], a[160:163], a[240:243], v[84:87]\n\t"
    "ds_write_b128 v194, v[152:155] offset:28800\n\t"
    "v_mfma_f32_16x16x32_bf16 v[60:63], a[166:169], a[132:135], 0\n\t"
    "v_sub_f32 v144, v96, v80\n\t"
    "v_sub_f32 v145, v97, v81\n\t"
    "v_mfma_f32_16x16x32_bf16 v[60:63], a[152:155], a[244:247], v[60:63]\n\t"
    "v_sub_f32 v146, v98, v82\n\t"
    "v_sub_f32 v147, v99, v83\n\t"
    "v_mfma_f32_16x16x32_bf16 v[60:63], a[156:159], a[248:251], v[60:63]\n\t"
    "ds_read_b128 a[144:147], v202 offset:13312\n\t"
    "v_cvt_pk_bf16_f32 v156, v144, v145\n\t"
    "v_mfma_f32_16x16x32_bf16 v[60:63], a[160:163], a[252:255], v[60:63]\n\t"
    "ds_read_b128 a[148:151], v202 offset:14336\n\t"
    "v_cvt_pk_bf16_f32 v157, v146, v147\n\t"
    "v_mfma_f32_16x16x32_bf16 v[96:99], a[164:167], a[124:127], 0\n\t"
    "ds_read_b128 a[152:155], v202 offset:15360\n\t"
    "v_sub_f32 v148, v100, v84\n\t"
    "v_mfma_f32_16x16x32_bf16 v[8:11], v[180:183], a[200:203], v[8:11]\n\t"
    "ds_read_b128 a[200:203], v190 offset:41536\n\t"
    "v_sub_f32 v149, v101, v85\n\t"
    "v_mfma_f32_16x16x32_bf16 v[8:11], v[52:55], a[204:207], v[8:11]\n\t"
    "ds_read_b128 a[204:207], v190 offset:41600\n\t"
    "v_sub_f32 v150, v102, v86\n\t"
    "s_waitcnt lgkmcnt(14)\n\t"
    "v_mfma_f32_16x16x32_bf16 v[12:15], a[108:111], a[172:175], 0\n\t"
    "ds_read_b128 a[156:159], v202 offset:16384\n\t"
    "v_sub_f32 v151, v103, v87\n\t"
    "v_mfma_f32_16x16x32_bf16 v[12:15], v[216:219], a[176:179], v[12:15]\n\t"
    "ds_read_b128 a[160:163], v202 offset:17408\n\t"
    "v_cvt_pk_bf16_f32 v158, v148, v149\n\t"
    "v_mfma_f32_16x16x32_bf16 v[12:15], v[228:231], a[180:183], v[12:15]\n\t"
    "ds_write_b128 v196, v[8:11] offset:14336\n\t"
    "v_mfma_f32_16x16x32_bf16 v[16:19], a[112:115], a[172:175], 0\n\t"
    "v_cvt_pk_bf16_f32 v159, v150, v151\n\t"
    "v_mfma_f32_16x16x32_bf16 v[16:19], v[220:223], a[176:179], v[16:19]\n\t"
    "v_mfma_f32_16x16x32_bf16 v[16:19], v[232:235], a[180:183], v[16:19]\n\t"
    "ds_write_b128 v194, v[156:159] offset:64\n\t"
    "v_mfma_f32_16x16x32_bf16 v[20:23], v[212:215], a[172:175], 0\n\t"
    "v_add_f32 v144, v72, v80\n\t"
    "v_add_f32 v145, v73, v81\n\t"
    "s_waitcnt lgkmcnt(7)\n\t"
    "v_mfma_f32_16x16x32_bf16 v[96:99], a[140:143], a[232:235], v[96:99]\n\t"
    "v_add_f32 v146, v74, v82\n\t"
    "v_add_f32 v147, v75, v83\n\t"
    "v_mfma_f32_16x16x32_bf16 v[96:99], a[144:147], a[236:239], v[96:99]\n\t"
    "ds_read_b128 a[172:175], v188 offset:44544\n\t"
    "v_cvt_pk_bf16_f32 v160, v144, v145\n\t"
    "v_mfma_f32_16x16x32_bf16 v[96:99], a[148:151], a[240:243], v[96:99]\n\t"
    "v_cvt_pk_bf16_f32 v161, v146, v147\n\t"
    "v_add_f32 v148, v76, v84\n\t"
    "v_mfma_f32_16x16x32_bf16 v[20:23], v[224:227], a[176:179], v[20:23]\n\t"
    "ds_read_b128 a[176:179], v188 offset:44608\n\t"
    "v_add_f32 v149, v77, v85\n\t"
    "v_mfma_f32_16x16x32_bf16 v[20:23], v[236:239], a[180:183], v[20:23]\n\t"
    "ds_read_b128 a[180:183], v188 offset:44672\n\t"
    "v_add_f32 v150, v78, v86\n\t"
    "v_mfma_f32_16x16x32_bf16 v[12:15], v[240:243], a[184:187], v[12:15]\n\t"
    "v_add_f32 v151, v79, v87\n\t"
    "v_cvt_pk_bf16_f32 v162, v148, v149\n\t"
    "v_mfma_f32_16x16x32_bf16 v[12:15], v[252:255], a[188:191], v[12:15]\n\t"
    "v_cvt_pk_bf16_f32 v163, v150, v151\n\t"
    "v_mfma_f32_16x16x32_bf16 v[12:15], v[128:131], a[192:195], v[12:15]\n\t"
    "ds_write_b128 v194, v[160:163] offset:9664\n\t"
    "v_mfma_f32_16x16x32_bf16 v[16:19], v[244:247], a[184:187], v[16:19]\n\t"
    "v_sub_f32 v144, v72, v80\n\t"
    "v_sub_f32 v145, v73, v81\n\t"
    "v_mfma_f32_16x16x32_bf16 v[16:19], v[120:123], a[188:191], v[16:19]\n\t"
    "v_sub_f32 v146, v74, v82\n\t"
    "v_sub_f32 v147, v75, v83\n\t"
    "v_mfma_f32_16x16x32_bf16 v[16:19], v[132:135], a[192:195], v[16:19]\n\t"
    "v_cvt_pk_bf16_f32 v152, v144, v145\n\t"
    "v_cvt_pk_bf16_f32 v153, v146, v147\n\t"
    "v_mfma_f32_16x16x32_bf16 v[20:23], v[248:251], a[184:187], v[20:23]\n\t"
    "ds_read_b128 a[184:187], v189 offset:44544\n\t"
    "v_sub_f32 v148, v76, v84\n\t"
    "v_mfma_f32_16x16x32_bf16 v[20:23], v[124:127], a[188:191], v[20:23]\n\t"
    "ds_read_b128 a[188:191], v189 offset:44608\n\t"
    "v_sub_f32 v149, v77, v85\n\t"
    "v_mfma_f32_16x16x32_bf16 v[20:23], v[136:139], a[192:195], v[20:23]\n\t"
    "ds_read_b128 a[192:195], v189 offset:44672\n\t"
    "v_sub_f32 v150, v78, v86\n\t"
    "s_waitcnt lgkmcnt(11)\n\t"
    "v_mfma_f32_16x16x32_bf16 v[12:15], v[140:143], a[196:199], v[12:15]\n\t"
    "v_sub_f32 v151, v79, v87\n\t"
    "v_cvt_pk_bf16_f32 v154, v148, v149\n\t"
    "v_mfma_f32_16x16x32_bf16 v[12:15], v[172:175], a[200:203], v[12:15]\n\t"
    "v_cvt_pk_bf16_f32 v155, v150, v151\n\t"
    "v_mfma_f32_16x16x32_bf16 v[12:15], v[184:187], a[204:207], v[12:15]\n\t"
    "ds_write_b128 v194, v[152:155] offset:19264\n\t"
    "v_mfma_f32_16x16x32_bf16 v[16:19], v[164:167], a[196:199], v[16:19]\n\t"
    "v_sub_f32 v144, v56, v72\n\t"
    "v_sub_f32 v145, v57, v73\n\t"
    "v_mfma_f32_16x16x32_bf16 v[16:19], v[176:179], a[200:203], v[16:19]\n\t"
    "v_sub_f32 v146, v58, v74\n\t"
    "v_sub_f32 v147, v59, v75\n\t"
    "s_waitcnt vmcnt(14)\n\t"
    "v_mfma_f32_16x16x32_bf16 v[72:75], a[164:167], a[136:139], 0\n\t"
    "ds_write_b128 v196, v[12:15] offset:24576\n\t"
    "v_mfma_f32_16x16x32_bf16 v[72:75], a[140:143], a[244:247], v[72:75]\n\t"
    "v_cvt_pk_bf16_f32 v156, v144, v145\n\t"
    "v_cvt_pk_bf16_f32 v157, v146, v147\n\t"
    "v_mfma_f32_16x16x32_bf16 v[72:75], a[144:147], a[248:251], v[72:75]\n\t"
    "v_sub_f32 v148, v60, v76\n\t"
    "v_sub_f32 v149, v61, v77\n\t"
    "v_mfma_f32_16x16x32_bf16 v[72:75], a[148:151], a[252:255], v[72:75]\n\t"
    "ds_read_b128 a[140:143], v202 offset:0\n\t"
    "v_sub_f32 v150, v62, v78\n\t"
    "v_mfma_f32_16x16x32_bf16 v[100:103], a[166:169], a[124:127], 0\n\t"
    "buffer_load_dwordx4 a[124:127], v199, s[40:43], s72 offen offset:128\n\t"
    "s_waitcnt lgkmcnt(12)\n\t"
    "v_mfma_f32_16x16x32_bf16 v[100:103], a[152:155], a[232:235], v[100:103]\n\t"
    "ds_read_b128 a[144:147], v202 offset:1024\n\t"
    "v_sub_f32 v151, v63, v79\n\t"
    "v_mfma_f32_16x16x32_bf16 v[100:103], a[156:159], a[236:239], v[100:103]\n\t"
    "ds_read_b128 a[148:151], v202 offset:2048\n\t"
    "v_cvt_pk_bf16_f32 v158, v148, v149\n\t"
    "v_mfma_f32_16x16x32_bf16 v[100:103], a[160:163], a[240:243], v[100:103]\n\t"
    "v_cvt_pk_bf16_f32 v159, v150, v151\n\t"
    "v_mfma_f32_16x16x32_bf16 v[76:79], a[166:169], a[136:139], 0\n\t"
    "ds_write_b128 v194, v[156:159] offset:28864\n\t"
    "v_mfma_f32_16x16x32_bf16 v[76:79], a[152:155], a[244:247], v[76:79]\n\t"
    "v_sub_f32 v144, v104, v96\n\t"
    "v_sub_f32 v145, v105, v97\n\t"
    "v_mfma_f32_16x16x32_bf16 v[76:79], a[156:159], a[248:251], v[76:79]\n\t"
    "v_sub_f32 v146, v106, v98\n\t"
    "v_sub_f32 v147, v107, v99\n\t"
    "v_mfma_f32_16x16x32_bf16 v[76:79], a[160:163], a[252:255], v[76:79]\n\t"
    "ds_read_b128 a[152:155], v202 offset:3072\n\t"
    "v_cvt_pk_bf16_f32 v160, v144, v145\n\t"
    "v_mfma_f32_16x16x32_bf16 v[104:107], a[164:167], a[128:131], 0\n\t"
    "ds_read_b128 a[156:159], v202 offset:4096\n\t"
    "v_cvt_pk_bf16_f32 v161, v146, v147\n\t"
    "v_mfma_f32_16x16x32_bf16 v[16:19], v[48:51], a[204:207], v[16:19]\n\t"
    "ds_read_b128 a[160:163], v202 offset:5120\n\t"
    "v_sub_f32 v148, v108, v100\n\t"
    "v_mfma_f32_16x16x32_bf16 v[20:23], v[168:171], a[196:199], v[20:23]\n\t"
    "ds_read_b128 a[196:199], v190 offset:44544\n\t"
    "v_sub_f32 v149, v109, v101\n\t"
    "v_mfma_f32_16x16x32_bf16 v[20:23], v[180:183], a[200:203], v[20:23]\n\t"
    "ds_read_b128 a[200:203], v190 offset:44608\n\t"
    "v_sub_f32 v150, v110, v102\n\t"
    "v_mfma_f32_16x16x32_bf16 v[20:23], v[52:55], a[204:207], v[20:23]\n\t"
    "ds_write_b128 v196, v[16:19] offset:25600\n\t"
    "s_waitcnt vmcnt(12) lgkmcnt(7)\n\t"
    "v_mfma_f32_16x16x32_bf16 v[104:107], a[140:143], a[208:211], v[104:107]\n\t"
    "v_sub_f32 v151, v111, v103\n\t"
    "v_cvt_pk_bf16_f32 v162, v148, v149\n\t"
    "v_mfma_f32_16x16x32_bf16 v[104:107], a[144:147], a[212:215], v[104:107]\n\t"
    "v_cvt_pk_bf16_f32 v163, v150, v151\n\t"
    "v_mfma_f32_16x16x32_bf16 v[104:107], a[148:151], a[216:219], v[104:107]\n\t"
    "ds_write_b128 v194, v[160:163] offset:128\n\t"
    "v_mfma_f32_16x16x32_bf16 v[24:27], a[108:111], a[172:175], 0\n\t"
    "v_add_f32 v144, v88, v96\n\t"
    "v_add_f32 v145, v89, v97\n\t"
    "v_mfma_f32_16x16x32_bf16 v[24:27], v[216:219], a[176:179], v[24:27]\n\t"
    "v_add_f32 v146, v90, v98\n\t"
    "v_add_f32 v147, v91, v99\n\t"
    "v_mfma_f32_16x16x32_bf16 v[24:27], v[228:231], a[180:183], v[24:27]\n\t"
    "ds_write_b128 v196, v[20:23] offset:26624\n\t"
    "v_mfma_f32_16x16x32_bf16 v[28:31], a[112:115], a[172:175], 0\n\t"
    "v_cvt_pk_bf16_f32 v152, v144, v145\n\t"
    "v_cvt_pk_bf16_f32 v153, v146, v147\n\t"
    "v_mfma_f32_16x16x32_bf16 v[28:31], v[220:223], a[176:179], v[28:31]\n\t"
    "v_add_f32 v148, v92, v100\n\t"
    "v_add_f32 v149, v93, v101\n\t"
    "v_mfma_f32_16x16x32_bf16 v[28:31], v[232:235], a[180:183], v[28:31]\n\t"
    "ds_read_b128 a[204:207], v190 offset:44672\n\t"
    "v_add_f32 v150, v94, v102\n\t"
    "v_mfma_f32_16x16x32_bf16 v[32:35], v[212:215], a[172:175], 0\n\t"
    "ds_read_b128 a[172:175], v191 offset:38400\n\t"
    "v_add_f32 v151, v95, v103\n\t"
    "v_mfma_f32_16x16x32_bf16 v[32:35], v[224:227], a[176:179], v[32:35]\n\t"
    "ds_read_b128 a[176:179], v191 offset:38464\n\t"
    "v_cvt_pk_bf16_f32 v154, v148, v149\n\t"
    "v_mfma_f32_16x16x32_bf16 v[32:35], v[236:239], a[180:183], v[32:35]\n\t"
    "ds_read_b128 a[180:183], v191 offset:38528\n\t"
    "v_cvt_pk_bf16_f32 v155, v150, v151\n\t"
    "v_mfma_f32_16x16x32_bf16 v[24:27], v[240:243], a[184:187], v[24:27]\n\t"
    "ds_write_b128 v194, v[152:155] offset:9728\n\t"
    "v_mfma_f32_16x16x32_bf16 v[24:27], v[252:255], a[188:191], v[24:27]\n\t"
    "v_sub_f32 v144, v88, v96\n\t"
    "v_sub_f32 v145, v89, v97\n\t"
    "v_mfma_f32_16x16x32_bf16 v[24:27], v[128:131], a[192:195], v[24:27]\n\t"
    "v_sub_f32 v146, v90, v98\n\t"
    "v_sub_f32 v147, v91, v99\n\t"
    "v_mfma_f32_16x16x32_bf16 v[28:31], v[244:247], a[184:187], v[28:31]\n\t"
    "v_cvt_pk_bf16_f32 v156, v144, v145\n\t"
    "v_cvt_pk_bf16_f32 v157, v146, v147\n\t"
    "v_mfma_f32_16x16x32_bf16 v[28:31], v[120:123], a[188:191], v[28:31]\n\t"
    "v_sub_f32 v148, v92, v100\n\t"
    "v_sub_f32 v149, v93, v101\n\t"
    "v_mfma_f32_16x16x32_bf16 v[28:31], v[132:135], a[192:195], v[28:31]\n\t"
    "v_sub_f32 v150, v94, v102\n\t"
    "v_sub_f32 v151, v95, v103\n\t"
    "v_mfma_f32_16x16x32_bf16 v[32:35], v[248:251], a[184:187], v[32:35]\n\t"
    "ds_read_b128 a[184:187], v192 offset:38400\n\t"
    "v_cvt_pk_bf16_f32 v158, v148, v149\n\t"
    "v_mfma_f32_16x16x32_bf16 v[32:35], v[124:127], a[188:191], v[32:35]\n\t"
    "ds_read_b128 a[188:191], v192 offset:38464\n\t"
    "v_cvt_pk_bf16_f32 v159, v150, v151\n\t"
    "v_mfma_f32_16x16x32_bf16 v[32:35], v[136:139], a[192:195], v[32:35]\n\t"
    "ds_write_b128 v194, v[156:159] offset:19328\n\t"
    "s_waitcnt lgkmcnt(7)\n\t"
    "v_mfma_f32_16x16x32_bf16 v[24:27], v[140:143], a[196:199], v[24:27]\n\t"
    "v_sub_f32 v144, v72, v88\n\t"
    "v_sub_f32 v145, v73, v89\n\t"
    "v_mfma_f32_16x16x32_bf16 v[24:27], v[172:175], a[200:203], v[24:27]\n\t"
    "v_sub_f32 v146, v74, v90\n\t"
    "v_sub_f32 v147, v75, v91\n\t"
    "s_waitcnt vmcnt(8)\n\t"
    "v_mfma_f32_16x16x32_bf16 v[88:91], a[164:167], a[116:119], 0\n\t"
    "ds_read_b128 a[192:195], v192 offset:38528\n\t"
    "v_cvt_pk_bf16_f32 v160, v144, v145\n\t"
    "v_mfma_f32_16x16x32_bf16 v[88:91], a[140:143], a[220:223], v[88:91]\n\t"
    "ds_read_b128 a[140:143], v202 offset:6144\n\t"
    "v_cvt_pk_bf16_f32 v161, v146, v147\n\t"
    "v_mfma_f32_16x16x32_bf16 v[88:91], a[144:147], a[224:227], v[88:91]\n\t"
    "ds_read_b128 a[144:147], v202 offset:7168\n\t"
    "v_sub_f32 v148, v76, v92\n\t"
    "v_mfma_f32_16x16x32_bf16 v[88:91], a[148:151], a[228:231], v[88:91]\n\t"
    "ds_read_b128 a[148:151], v202 offset:8192\n\t"
    "v_sub_f32 v149, v77, v93\n\t"
    "v_mfma_f32_16x16x32_bf16 v[108:111], a[166:169], a[128:131], 0\n\t"
    "v_sub_f32 v150, v78, v94\n\t"
    "v_sub_f32 v151, v79, v95\n\t"
    "v_mfma_f32_16x16x32_bf16 v[108:111], a[152:155], a[208:211], v[108:111]\n\t"
    "v_cvt_pk_bf16_f32 v162, v148, v149\n\t"
    "v_cvt_pk_bf16_f32 v163, v150, v151\n\t"
    "v_mfma_f32_16x16x32_bf16 v[108:111], a[156:159], a[212:215], v[108:111]\n\t"
    "ds_write_b128 v194, v[160:163] offset:28928\n\t"
    "v_mfma_f32_16x16x32_bf16 v[108:111], a[160:163], a[216:219], v[108:111]\n\t"
    "v_sub_f32 v144, v64, v104\n\t"
    "v_sub_f32 v145, v65, v105\n\t"
    "v_mfma_f32_16x16x32_bf16 v[92:95], a[166:169], a[116:119], 0\n\t"
    "v_sub_f32 v146, v66, v106\n\t"
    "v_sub_f32 v147, v67, v107\n\t"
    "v_mfma_f32_16x16x32_bf16 v[92:95], a[152:155], a[220:223], v[92:95]\n\t"
    "ds_read_b128 a[152:155], v202 offset:9216\n\t"
    "v_cvt_pk_bf16_f32 v152, v144, v145\n\t"
    "v_mfma_f32_16x16x32_bf16 v[92:95], a[156:159], a[224:227], v[92:95]\n\t"
    "ds_read_b128 a[156:159], v202 offset:10240\n\t"
    "v_cvt_pk_bf16_f32 v153, v146, v147\n\t"
    "v_mfma_f32_16x16x32_bf16 v[92:95], a[160:163], a[228:231], v[92:95]\n\t"
    "ds_read_b128 a[160:163], v202 offset:11264\n\t"
    "v_sub_f32 v148, v68, v108\n\t"
    "v_mfma_f32_16x16x32_bf16 v[64:67], a[164:167], a[132:135], 0\n\t"
    "v_sub_f32 v149, v69, v109\n\t"
    "v_sub_f32 v150, v70, v110\n\t"
    "v_mfma_f32_16x16x32_bf16 v[24:27], v[184:187], a[204:207], v[24:27]\n\t"
    "v_sub_f32 v151, v71, v111\n\t"
    "v_cvt_pk_bf16_f32 v154, v148, v149\n\t"
    "s_waitcnt lgkmcnt(4)\n\t"
    "v_mfma_f32_16x16x32_bf16 v[64:67], a[140:143], a[208:211], v[64:67]\n\t"
    "v_cvt_pk_bf16_f32 v155, v150, v151\n\t"
    "v_mfma_f32_16x16x32_bf16 v[64:67], a[144:147], a[212:215], v[64:67]\n\t"
    "v_mfma_f32_16x16x32_bf16 v[64:67], a[148:151], a[216:219], v[64:67]\n\t"
    "v_mfma_f32_16x16x32_bf16 v[28:31], v[164:167], a[196:199], v[28:31]\n\t"
    "v_mfma_f32_16x16x32_bf16 v[28:31], v[176:179], a[200:203], v[28:31]\n\t"
    "ds_write_b128 v196, v[24:27] offset:36864\n\t"
    "v_mfma_f32_16x16x32_bf16 v[28:31], v[48:51], a[204:207], v[28:31]\n\t"
    "v_mfma_f32_16x16x32_bf16 v[32:35], v[168:171], a[196:199], v[32:35]\n\t"
    "v_mfma_f32_16x16x32_bf16 v[32:35], v[180:183], a[200:203], v[32:35]\n\t"
    "ds_read_b128 a[196:199], v193 offset:38400\n\t"
    "v_mfma_f32_16x16x32_bf16 v[32:35], v[52:55], a[204:207], v[32:35]\n\t"
    "ds_read_b128 a[200:203], v193 offset:38464\n\t"
    "v_mfma_f32_16x16x32_bf16 v[0:3], a[0:3], a[172:175], v[0:3]\n\t"
    "ds_read_b128 a[204:207], v193 offset:38528\n\t"
    "v_mfma_f32_16x16x32_bf16 v[0:3], a[12:15], a[176:179], v[0:3]\n\t"
    "ds_write_b128 v196, v[28:31] offset:37888\n\t"
    "v_mfma_f32_16x16x32_bf16 v[0:3], a[24:27], a[180:183], v[0:3]\n\t"
    "v_mfma_f32_16x16x32_bf16 v[4:7], a[4:7], a[172:175], v[4:7]\n\t"
    "v_mfma_f32_16x16x32_bf16 v[4:7], a[16:19], a[176:179], v[4:7]\n\t"
    "ds_write_b128 v196, v[32:35] offset:38912\n\t"
    "v_mfma_f32_16x16x32_bf16 v[4:7], a[28:31], a[180:183], v[4:7]\n\t"
    "v_mfma_f32_16x16x32_bf16 v[8:11], a[8:11], a[172:175], v[8:11]\n\t"
    "v_mfma_f32_16x16x32_bf16 v[8:11], a[20:23], a[176:179], v[8:11]\n\t"
    "ds_read_b128 a[172:175], v191 offset:41472\n\t"
    "v_mfma_f32_16x16x32_bf16 v[8:11], a[32:35], a[180:183], v[8:11]\n\t"
    "ds_read_b128 a[176:179], v191 offset:41536\n\t"
    "ds_read_b128 a[180:183], v191 offset:41600\n\t"
    "s_waitcnt lgkmcnt(3)\n\t"
    "s_barrier\n\t"
    "v_mfma_f32_16x16x32_bf16 v[0:3], a[36:39], a[184:187], v[0:3]\n\t"
    "ds_read_b128 v[36:39], v197 offset:12288\n\t"
    "v_mfma_f32_16x16x32_bf16 v[0:3], a[48:51], a[188:191], v[0:3]\n\t"
    "ds_read_b128 v[40:43], v197 offset:13312\n\t"
    "v_mfma_f32_16x16x32_bf16 v[0:3], a[60:63], a[192:195], v[0:3]\n\t"
    "ds_read_b128 v[44:47], v197 offset:14336\n\t"
    "s_add_u32 s77, s74, 0x6c000\n\t"
    "v_mfma_f32_16x16x32_bf16 v[4:7], a[40:43], a[184:187], v[4:7]\n\t"
    "ds_read_b64 a[170:171], v211 offset:2048\n\t"
    "s_add_u32 s86, s74, 0x48c00\n\t"
    "v_mfma_f32_16x16x32_bf16 v[4:7], a[52:55], a[188:191], v[4:7]\n\t"
    "v_mfma_f32_16x16x32_bf16 v[4:7], a[64:67], a[192:195], v[4:7]\n\t"
    "v_mfma_f32_16x16x32_bf16 v[8:11], a[44:47], a[184:187], v[8:11]\n\t"
    "ds_read_b128 a[184:187], v192 offset:41472\n\t"
    "v_mfma_f32_16x16x32_bf16 v[8:11], a[56:59], a[188:191], v[8:11]\n\t"
    "ds_read_b128 a[188:191], v192 offset:41536\n\t"
    "v_mfma_f32_16x16x32_bf16 v[8:11], a[68:71], a[192:195], v[8:11]\n\t"
    "ds_read_b128 a[192:195], v192 offset:41600\n\t"
    "v_mfma_f32_16x16x32_bf16 v[0:3], a[72:75], a[196:199], v[0:3]\n\t"
    "v_mfma_f32_16x16x32_bf16 v[0:3], a[84:87], a[200:203], v[0:3]\n\t"
    "v_mfma_f32_16x16x32_bf16 v[0:3], a[96:99], a[204:207], v[0:3]\n\t"
    "v_mfma_f32_16x16x32_bf16 v[4:7], a[76:79], a[196:199], v[4:7]\n\t"
    "v_mfma_f32_16x16x32_bf16 v[4:7], a[88:91], a[200:203], v[4:7]\n\t"
    "v_mfma_f32_16x16x32_bf16 v[4:7], a[100:103], a[204:207], v[4:7]\n\t"
    "v_mfma_f32_16x16x32_bf16 v[8:11], a[80:83], a[196:199], v[8:11]\n\t"
    "s_waitcnt lgkmcnt(3)\n\t"
    "buffer_store_dwordx2 a[170:171], v201, s[52:55], s86 offen offset:0 sc1\n\t"
    "v_mfma_f32_16x16x32_bf16 v[8:11], a[92:95], a[200:203], v[8:11]\n\t"
    "ds_read_b128 a[196:199], v193 offset:41472\n\t"
    "v_mfma_f32_16x16x32_bf16 v[8:11], a[104:107], a[204:207], v[8:11]\n\t"
    "ds_read_b128 a[200:203], v193 offset:41536\n\t"
    "v_fma_f32 v36, v36, s75, v0\n\t"
    "v_mfma_f32_16x16x32_bf16 v[12:15], a[0:3], a[172:175], v[12:15]\n\t"
    "ds_read_b128 a[204:207], v193 offset:41600\n\t"
    "v_fma_f32 v37, v37, s75, v1\n\t"
    "v_mfma_f32_16x16x32_bf16 v[12:15], a[12:15], a[176:179], v[12:15]\n\t"
    "v_fma_f32 v38, v38, s75, v2\n\t"
    "v_fma_f32 v39, v39, s75, v3\n\t"
    "v_mfma_f32_16x16x32_bf16 v[12:15], a[24:27], a[180:183], v[12:15]\n\t"
    "v_cvt_pk_bf16_f32 v36, v36, v37\n\t"
    "v_cvt_pk_bf16_f32 v37, v38, v39\n\t"
    "v_mfma_f32_16x16x32_bf16 v[16:19], a[4:7], a[172:175], v[16:19]\n\t"
    "v_fma_f32 v40, v40, s75, v4\n\t"
    "v_fma_f32 v41, v41, s75, v5\n\t"
    "v_mfma_f32_16x16x32_bf16 v[16:19], a[16:19], a[176:179], v[16:19]\n\t"
    "v_fma_f32 v42, v42, s75, v6\n\t"
    "v_fma_f32 v43, v43, s75, v7\n\t"
    "v_mfma_f32_16x16x32_bf16 v[16:19], a[28:31], a[180:183], v[16:19]\n\t"
    "v_cvt_pk_bf16_f32 v38, v40, v41\n\t"
    "v_cvt_pk_bf16_f32 v39, v42, v43\n\t"
    "v_mfma_f32_16x16x32_bf16 v[20:23], a[8:11], a[172:175], v[20:23]\n\t"
    "buffer_store_dwordx4 v[36:39], v200, s[52:55], s77 offen offset:0 sc1\n\t"
    "v_mfma_f32_16x16x32_bf16 v[20:23], a[20:23], a[176:179], v[20:23]\n\t"
    "ds_read_b128 a[172:175], v191 offset:44544\n\t"
    "v_fma_f32 v44, v44, s75, v8\n\t"
    "v_mfma_f32_16x16x32_bf16 v[20:23], a[32:35], a[180:183], v[20:23]\n\t"
    "ds_read_b128 a[176:179], v191 offset:44608\n\t"
    "v_fma_f32 v45, v45, s75, v9\n\t"
    "s_waitcnt lgkmcnt(5)\n\t"
    "v_mfma_f32_16x16x32_bf16 v[12:15], a[36:39], a[184:187], v[12:15]\n\t"
    "ds_read_b128 a[180:183], v191 offset:44672\n\t"
    "v_fma_f32 v46, v46, s75, v10\n\t"
    "v_mfma_f32_16x16x32_bf16 v[12:15], a[48:51], a[188:191], v[12:15]\n\t"
    "v_fma_f32 v47, v47, s75, v11\n\t"
    "v_cvt_pk_bf16_f32 v44, v44, v45\n\t"
    "v_mfma_f32_16x16x32_bf16 v[12:15], a[60:63], a[192:195], v[12:15]\n\t"
    "v_cvt_pk_bf16_f32 v45, v46, v47\n\t"
    "ds_read_b64 a[170:171], v211 offset:4096\n\t"
    "s_add_u32 s86, s74, 0x49800\n\t"
    "v_mfma_f32_16x16x32_bf16 v[16:19], a[40:43], a[184:187], v[16:19]\n\t"
    "ds_write_b64 v210, v[44:45] offset:6144\n\t"
    "v_mfma_f32_16x16x32_bf16 v[16:19], a[52:55], a[188:191], v[16:19]\n\t"
    "v_mfma_f32_16x16x32_bf16 v[16:19], a[64:67], a[192:195], v[16:19]\n\t"
    "ds_read_b128 v[36:39], v197 offset:24576\n\t"
    "v_mfma_f32_16x16x32_bf16 v[20:23], a[44:47], a[184:187], v[20:23]\n\t"
    "ds_read_b128 a[184:187], v192 offset:44544\n\t"
    "v_mfma_f32_16x16x32_bf16 v[20:23], a[56:59], a[188:191], v[20:23]\n\t"
    "ds_read_b128 a[188:191], v192 offset:44608\n\t"
    "v_mfma_f32_16x16x32_bf16 v[20:23], a[68:71], a[192:195], v[20:23]\n\t"
    "ds_read_b128 a[192:195], v192 offset:44672\n\t"
    "s_waitcnt lgkmcnt(9)\n\t"
    "v_mfma_f32_16x16x32_bf16 v[12:15], a[72:75], a[196:199], v[12:15]\n\t"
    "ds_read_b128 v[40:43], v197 offset:25600\n\t"
    "v_mfma_f32_16x16x32_bf16 v[12:15], a[84:87], a[200:203], v[12:15]\n\t"
    "ds_read_b128 v[44:47], v197 offset:26624\n\t"
    "s_add_u32 s76, s74, 0x6cc00\n\t"
    "v_mfma_f32_16x16x32_bf16 v[12:15], a[96:99], a[204:207], v[12:15]\n\t"
    "v_mfma_f32_16x16x32_bf16 v[16:19], a[76:79], a[196:199], v[16:19]\n\t"
    "v_mfma_f32_16x16x32_bf16 v[16:19], a[88:91], a[200:203], v[16:19]\n\t"
    "v_mfma_f32_16x16x32_bf16 v[16:19], a[100:103], a[204:207], v[16:19]\n\t"
    "s_waitcnt lgkmcnt(7)\n\t"
    "buffer_store_dwordx2 a[170:171], v201, s[52:55], s86 offen offset:0 sc1\n\t"
    "v_mfma_f32_16x16x32_bf16 v[20:23], a[80:83], a[196:199], v[20:23]\n\t"
    "ds_read_b128 a[196:199], v193 offset:44544\n\t"
    "v_mfma_f32_16x16x32_bf16 v[20:23], a[92:95], a[200:203], v[20:23]\n\t"
    "ds_read_b128 a[200:203], v193 offset:44608\n\t"
    "v_mfma_f32_16x16x32_bf16 v[20:23], a[104:107], a[204:207], v[20:23]\n\t"
    "ds_read_b128 a[204:207], v193 offset:44672\n\t"
    "v_mfma_f32_16x16x32_bf16 v[24:27], a[0:3], a[172:175], v[24:27]\n\t"
    "v_mfma_f32_16x16x32_bf16 v[24:27], a[12:15], a[176:179], v[24:27]\n\t"
    "v_mfma_f32_16x16x32_bf16 v[24:27], a[24:27], a[180:183], v[24:27]\n\t"
    "v_mfma_f32_16x16x32_bf16 v[28:31], a[4:7], a[172:175], v[28:31]\n\t"
    "s_waitcnt lgkmcnt(3)\n\t"
    "v_fma_f32 v36, v36, s75, v12\n\t"
    "v_mfma_f32_16x16x32_bf16 v[28:31], a[16:19], a[176:179], v[28:31]\n\t"
    "v_fma_f32 v37, v37, s75, v13\n\t"
    "v_fma_f32 v38, v38, s75, v14\n\t"
    "v_mfma_f32_16x16x32_bf16 v[28:31], a[28:31], a[180:183], v[28:31]\n\t"
    "v_fma_f32 v39, v39, s75, v15\n\t"
    "v_cvt_pk_bf16_f32 v36, v36, v37\n\t"
    "v_mfma_f32_16x16x32_bf16 v[32:35], a[8:11], a[172:175], v[32:35]\n\t"
    "v_cvt_pk_bf16_f32 v37, v38, v39\n\t"
    "v_fma_f32 v40, v40, s75, v16\n\t"
    "v_mfma_f32_16x16x32_bf16 v[32:35], a[20:23], a[176:179], v[32:35]\n\t"
    "v_fma_f32 v41, v41, s75, v17\n\t"
    "v_fma_f32 v42, v42, s75, v18\n\t"
    "v_mfma_f32_16x16x32_bf16 v[32:35], a[32:35], a[180:183], v[32:35]\n\t"
    "v_fma_f32 v43, v43, s75, v19\n\t"
    "v_cvt_pk_bf16_f32 v38, v40, v41\n\t"
    "v_cvt_pk_bf16_f32 v39, v42, v43\n\t"
    "buffer_store_dwordx4 v[36:39], v200, s[52:55], s76 offen offset:0 sc1\n\t"
    "v_fma_f32 v44, v44, s75, v20\n\t"
    "s_waitcnt lgkmcnt(0)\n\t"
    "s_barrier\n\t"
    "v_mfma_f32_16x16x32_bf16 v[24:27], a[36:39], a[184:187], v[24:27]\n\t"
    "ds_write_b128 v195, v[152:155] offset:0\n\t"
    "v_mfma_f32_16x16x32_bf16 v[24:27], a[48:51], a[188:191], v[24:27]\n\t"
    "v_fma_f32 v45, v45, s75, v21\n\t"
    "v_fma_f32 v46, v46, s75, v22\n\t"
    "v_mfma_f32_16x16x32_bf16 v[24:27], a[60:63], a[192:195], v[24:27]\n\t"
    "v_fma_f32 v47, v47, s75, v23\n\t"
    "v_cvt_pk_bf16_f32 v44, v44, v45\n\t"
    "v_mfma_f32_16x16x32_bf16 v[28:31], a[40:43], a[184:187], v[28:31]\n\t"
    "ds_read_b128 a[172:175], v188 offset:0\n\t"
    "v_cvt_pk_bf16_f32 v45, v46, v47\n\t"
    "v_mfma_f32_16x16x32_bf16 v[28:31], a[52:55], a[188:191], v[28:31]\n\t"
    "ds_write_b64 v210, v[44:45] offset:8192\n\t"
    "v_mfma_f32_16x16x32_bf16 v[28:31], a[64:67], a[192:195], v[28:31]\n\t"
    "v_add_f32 v144, v112, v104\n\t"
    "v_add_f32 v145, v113, v105\n\t"
    "v_mfma_f32_16x16x32_bf16 v[32:35], a[44:47], a[184:187], v[32:35]\n\t"
    "ds_read_b128 a[176:179], v188 offset:64\n\t"
    "v_add_f32 v146, v114, v106\n\t"
    "v_mfma_f32_16x16x32_bf16 v[32:35], a[56:59], a[188:191], v[32:35]\n\t"
    "ds_read_b128 a[180:183], v188 offset:128\n\t"
    "v_add_f32 v147, v115, v107\n\t"
    "v_mfma_f32_16x16x32_bf16 v[32:35], a[68:71], a[192:195], v[32:35]\n\t"
    "ds_read_b128 a[184:187], v189 offset:0\n\t"
    "v_cvt_pk_bf16_f32 v156, v144, v145\n\t"
    "v_mfma_f32_16x16x32_bf16 v[24:27], a[72:75], a[196:199], v[24:27]\n\t"
    "ds_read_b128 a[188:191], v189 offset:64\n\t"
    "v_cvt_pk_bf16_f32 v157, v146, v147\n\t"
    "v_mfma_f32_16x16x32_bf16 v[24:27], a[84:87], a[200:203], v[24:27]\n\t"
    "ds_read_b128 a[192:195], v189 offset:128\n\t"
    "v_add_f32 v148, v116, v108\n\t"
    "v_mfma_f32_16x16x32_bf16 v[24:27], a[96:99], a[204:207], v[24:27]\n\t"
    "ds_read_b128 v[36:39], v197 offset:36864\n\t"
    "v_add_f32 v149, v117, v109\n\t"
    "v_mfma_f32_16x16x32_bf16 v[28:31], a[76:79], a[196:199], v[28:31]\n\t"
    "ds_read_b128 v[40:43], v197 offset:37888\n\t"
    "v_add_f32 v150, v118, v110\n\t"
    "v_mfma_f32_16x16x32_bf16 v[28:31], a[88:91], a[200:203], v[28:31]\n\t"
    "ds_read_b128 v[44:47], v197 offset:38912\n\t"
    "s_add_u32 s77, s74, 0x6d800\n\t"
    "v_add_f32 v151, v119, v111\n\t"
    "v_mfma_f32_16x16x32_bf16 v[28:31], a[100:103], a[204:207], v[28:31]\n\t"
    "ds_read_b64 a[170:171], v211 offset:6144\n\t"
    "s_add_u32 s86, s74, 0x6c000\n\t"
    "v_cvt_pk_bf16_f32 v158, v148, v149\n\t"
    "v_mfma_f32_16x16x32_bf16 v[32:35], a[80:83], a[196:199], v[32:35]\n\t"
    "ds_read_b128 a[196:199], v190 offset:0\n\t"
    "v_cvt_pk_bf16_f32 v159, v150, v151\n\t"
    "v_mfma_f32_16x16x32_bf16 v[32:35], a[92:95], a[200:203], v[32:35]\n\t"
    "ds_write_b128 v195, v[156:159] offset:9600\n\t"
    "v_mfma_f32_16x16x32_bf16 v[32:35], a[104:107], a[204:207], v[32:35]\n\t"
    "v_sub_f32 v144, v112, v104\n\t"
    "v_sub_f32 v145, v113, v105\n\t"
    "v_sub_f32 v146, v114, v106\n\t"
    "s_waitcnt lgkmcnt(9)\n\t"
    "v_mfma_f32_16x16x32_bf16 v[0:3], a[108:111], a[172:175], 0\n\t"
    "v_sub_f32 v147, v115, v107\n\t"
    "v_cvt_pk_bf16_f32 v160, v144, v145\n\t"
    "v_mfma_f32_16x16x32_bf16 v[0:3], v[216:219], a[176:179], v[0:3]\n\t"
    "ds_read_b128 a[200:203], v190 offset:64\n\t"
    "v_cvt_pk_bf16_f32 v161, v146, v147\n\t"
    "v_mfma_f32_16x16x32_bf16 v[0:3], v[228:231], a[180:183], v[0:3]\n\t"
    "ds_read_b128 a[204:207], v190 offset:128\n\t"
    "v_sub_f32 v148, v116, v108\n\t"
    "v_mfma_f32_16x16x32_bf16 v[4:7], a[112:115], a[172:175], 0\n\t"
    "v_sub_f32 v149, v117, v109\n\t"
    "v_sub_f32 v150, v118, v110\n\t"
    "v_mfma_f32_16x16x32_bf16 v[4:7], v[220:223], a[176:179], v[4:7]\n\t"
    "v_sub_f32 v151, v119, v111\n\t"
    "v_cvt_pk_bf16_f32 v162, v148, v149\n\t"
    "v_mfma_f32_16x16x32_bf16 v[4:7], v[232:235], a[180:183], v[4:7]\n\t"
    "v_cvt_pk_bf16_f32 v163, v150, v151\n\t"
    "v_mfma_f32_16x16x32_bf16 v[8:11], v[212:215], a[172:175], 0\n\t"
    "ds_write_b128 v195, v[160:163] offset:19200\n\t"
    "v_mfma_f32_16x16x32_bf16 v[8:11], v[224:227], a[176:179], v[8:11]\n\t"
    "s_waitcnt lgkmcnt(5)\n\t"
    "buffer_store_dwordx2 a[170:171], v201, s[52:55], s86 offen offset:0 sc1\n\t"
    "v_mfma_f32_16x16x32_bf16 v[8:11], v[236:239], a[180:183], v[8:11]\n\t"
    "v_fma_f32 v36, v36, s75, v24\n\t"
    "v_fma_f32 v37, v37, s75, v25\n\t"
    "v_mfma_f32_16x16x32_bf16 v[0:3], v[240:243], a[184:187], v[0:3]\n\t"
    "ds_read_b128 a[172:175], v188 offset:3072\n\t"
    "v_fma_f32 v38, v38, s75, v26\n\t"
    "v_mfma_f32_16x16x32_bf16 v[0:3], v[252:255], a[188:191], v[0:3]\n\t"
    "ds_read_b128 a[176:179], v188 offset:3136\n\t"
    "v_fma_f32 v39, v39, s75, v27\n\t"
    "v_mfma_f32_16x16x32_bf16 v[0:3], v[128:131], a[192:195], v[0:3]\n\t"
    "ds_read_b128 a[180:183], v188 offset:3200\n\t"
    "v_cvt_pk_bf16_f32 v36, v36, v37\n\t"
    "v_mfma_f32_16x16x32_bf16 v[4:7], v[244:247], a[184:187], v[4:7]\n\t"
    "v_cvt_pk_bf16_f32 v37, v38, v39\n\t"
    "v_fma_f32 v40, v40, s75, v28\n\t"
    "v_mfma_f32_16x16x32_bf16 v[4:7], v[120:123], a[188:191], v[4:7]\n\t"
    "v_fma_f32 v41, v41, s75, v29\n\t"
    "v_fma_f32 v42, v42, s75, v30\n\t"
    "v_mfma_f32_16x16x32_bf16 v[4:7], v[132:135], a[192:195], v[4:7]\n\t"
    "v_fma_f32 v43, v43, s75, v31\n\t"
    "v_cvt_pk_bf16_f32 v38, v40, v41\n\t"
    "v_mfma_f32_16x16x32_bf16 v[8:11], v[248:251], a[184:187], v[8:11]\n\t"
    "ds_read_b128 a[184:187], v189 offset:3072\n\t"
    "v_cvt_pk_bf16_f32 v39, v42, v43\n\t"
    "v_mfma_f32_16x16x32_bf16 v[8:11], v[124:127], a[188:191], v[8:11]\n\t"
    "buffer_store_dwordx4 v[36:39], v200, s[52:55], s77 offen offset:0 sc1\n\t"
    "v_mfma_f32_16x16x32_bf16 v[8:11], v[136:139], a[192:195], v[8:11]\n\t"
    "ds_read_b128 a[188:191], v189 offset:3136\n\t"
    "v_fma_f32 v44, v44, s75, v32\n\t"
    "s_waitcnt lgkmcnt(6)\n\t"
    "v_mfma_f32_16x16x32_bf16 v[0:3], v[140:143], a[196:199], v[0:3]\n\t"
    "ds_read_b128 a[192:195], v189 offset:3200\n\t"
    "v_fma_f32 v45, v45, s75, v33\n\t"
    "v_mfma_f32_16x16x32_bf16 v[0:3], v[172:175], a[200:203], v[0:3]\n\t"
    "v_fma_f32 v46, v46, s75, v34\n\t"
    "v_fma_f32 v47, v47, s75, v35\n\t"
    "v_mfma_f32_16x16x32_bf16 v[0:3], v[184:187], a[204:207], v[0:3]\n\t"
    "v_cvt_pk_bf16_f32 v44, v44, v45\n\t"
    "v_cvt_pk_bf16_f32 v45, v46, v47\n\t"
    "v_mfma_f32_16x16x32_bf16 v[4:7], v[164:167], a[196:199], v[4:7]\n\t"
    "ds_write_b64 v210, v[44:45] offset:10240\n\t"
    "v_mfma_f32_16x16x32_bf16 v[4:7], v[176:179], a[200:203], v[4:7]\n\t"
    "v_sub_f32 v144, v88, v112\n\t"
    "v_sub_f32 v145, v89, v113\n\t"
    "v_mfma_f32_16x16x32_bf16 v[4:7], v[48:51], a[204:207], v[4:7]\n\t"
    "ds_write_b128 v196, v[0:3] offset:0\n\t"
    "v_mfma_f32_16x16x32_bf16 v[8:11], v[168:171], a[196:199], v[8:11]\n\t"
    "v_sub_f32 v146, v90, v114\n\t"
    "v_sub_f32 v147, v91, v115\n\t"
    "s_waitcnt vmcnt(7)\n\t"
    "v_mfma_f32_16x16x32_bf16 v[112:115], a[164:167], a[120:123], 0\n\t"
    "v_cvt_pk_bf16_f32 v152, v144, v145\n\t"
    "v_cvt_pk_bf16_f32 v153, v146, v147\n\t"
    "v_mfma_f32_16x16x32_bf16 v[112:115], a[140:143], a[220:223], v[112:115]\n\t"
    "ds_write_b128 v196, v[4:7] offset:1024\n\t"
    "v_mfma_f32_16x16x32_bf16 v[112:115], a[144:147], a[224:227], v[112:115]\n\t"
    "v_sub_f32 v148, v92, v116\n\t"
    "v_sub_f32 v149, v93, v117\n\t"
    "v_mfma_f32_16x16x32_bf16 v[112:115], a[148:151], a[228:231], v[112:115]\n\t"
    "v_sub_f32 v150, v94, v118\n\t"
    "v_sub_f32 v151, v95, v119\n\t"
    "v_mfma_f32_16x16x32_bf16 v[68:71], a[166:169], a[132:135], 0\n\t"
    "ds_read_b128 a[196:199], v190 offset:3072\n\t"
    "v_cvt_pk_bf16_f32 v154, v148, v149\n\t"
    "v_mfma_f32_16x16x32_bf16 v[68:71], a[152:155], a[208:211], v[68:71]\n\t"
    "ds_read_b128 a[140:143], v202 offset:12288\n\t"
    "v_cvt_pk_bf16_f32 v155, v150, v151\n\t"
    "v_mfma_f32_16x16x32_bf16 v[68:71], a[156:159], a[212:215], v[68:71]\n\t"
    "ds_write_b128 v195, v[152:155] offset:28800\n\t"
    "v_mfma_f32_16x16x32_bf16 v[68:71], a[160:163], a[216:219], v[68:71]\n\t"
    "v_sub_f32 v144, v80, v64\n\t"
    "v_sub_f32 v145, v81, v65\n\t"
    "v_mfma_f32_16x16x32_bf16 v[116:119], a[166:169], a[120:123], 0\n\t"
    "v_sub_f32 v146, v82, v66\n\t"
    "v_sub_f32 v147, v83, v67\n\t"
    "v_mfma_f32_16x16x32_bf16 v[116:119], a[152:155], a[220:223], v[116:119]\n\t"
    "ds_read_b128 a[144:147], v202 offset:13312\n\t"
    "v_cvt_pk_bf16_f32 v156, v144, v145\n\t"
    "v_mfma_f32_16x16x32_bf16 v[116:119], a[156:159], a[224:227], v[116:119]\n\t"
    "ds_read_b128 a[148:151], v202 offset:14336\n\t"
    "v_cvt_pk_bf16_f32 v157, v146, v147\n\t"
    "v_mfma_f32_16x16x32_bf16 v[116:119], a[160:163], a[228:231], v[116:119]\n\t"
    "ds_read_b128 a[152:155], v202 offset:15360\n\t"
    "v_sub_f32 v148, v84, v68\n\t"
    "v_mfma_f32_16x16x32_bf16 v[80:83], a[164:167], a[136:139], 0\n\t"
    "ds_read_b128 a[156:159], v202 offset:16384\n\t"
    "v_sub_f32 v149, v85, v69\n\t"
    "v_mfma_f32_16x16x32_bf16 v[8:11], v[180:183], a[200:203], v[8:11]\n\t"
    "ds_read_b128 a[200:203], v190 offset:3136\n\t"
    "v_sub_f32 v150, v86, v70\n\t"
    "v_mfma_f32_16x16x32_bf16 v[8:11], v[52:55], a[204:207], v[8:11]\n\t"
    "ds_read_b128 a[204:207], v190 offset:3200\n\t"
    "v_sub_f32 v151, v87, v71\n\t"
    "s_waitcnt lgkmcnt(15)\n\t"
    "v_mfma_f32_16x16x32_bf16 v[12:15], a[108:111], a[172:175], 0\n\t"
    "ds_read_b128 a[160:163], v202 offset:17408\n\t"
    "v_cvt_pk_bf16_f32 v158, v148, v149\n\t"
    "v_mfma_f32_16x16x32_bf16 v[12:15], v[216:219], a[176:179], v[12:15]\n\t"
    "v_cvt_pk_bf16_f32 v159, v150, v151\n\t"
    "v_mfma_f32_16x16x32_bf16 v[12:15], v[228:231], a[180:183], v[12:15]\n\t"
    "ds_write_b128 v196, v[8:11] offset:2048\n\t"
    "v_mfma_f32_16x16x32_bf16 v[16:19], a[112:115], a[172:175], 0\n\t"
    "v_mfma_f32_16x16x32_bf16 v[16:19], v[220:223], a[176:179], v[16:19]\n\t"
    "v_mfma_f32_16x16x32_bf16 v[16:19], v[232:235], a[180:183], v[16:19]\n\t"
    "ds_write_b128 v195, v[156:159] offset:64\n\t"
    "s_waitcnt lgkmcnt(7)\n\t"
    "v_mfma_f32_16x16x32_bf16 v[80:83], a[140:143], a[208:211], v[80:83]\n\t"
    "v_add_f32 v144, v56, v64\n\t"
    "v_add_f32 v145, v57, v65\n\t"
    "v_mfma_f32_16x16x32_bf16 v[80:83], a[144:147], a[212:215], v[80:83]\n\t"
    "v_add_f32 v146, v58, v66\n\t"
    "v_add_f32 v147, v59, v67\n\t"
    "v_mfma_f32_16x16x32_bf16 v[80:83], a[148:151], a[216:219], v[80:83]\n\t"
    "v_cvt_pk_bf16_f32 v160, v144, v145\n\t"
    "v_cvt_pk_bf16_f32 v161, v146, v147\n\t"
    "v_mfma_f32_16x16x32_bf16 v[20:23], v[212:215], a[172:175], 0\n\t"
    "ds_read_b128 a[172:175], v188 offset:6144\n\t"
    "v_add_f32 v148, v60, v68\n\t"
    "v_mfma_f32_16x16x32_bf16 v[20:23], v[224:227], a[176:179], v[20:23]\n\t"
    "ds_read_b128 a[176:179], v188 offset:6208\n\t"
    "v_add_f32 v149, v61, v69\n\t"
    "v_mfma_f32_16x16x32_bf16 v[20:23], v[236:239], a[180:183], v[20:23]\n\t"
    "ds_read_b128 a[180:183], v188 offset:6272\n\t"
    "v_add_f32 v150, v62, v70\n\t"
    "v_mfma_f32_16x16x32_bf16 v[12:15], v[240:243], a[184:187], v[12:15]\n\t"
    "v_add_f32 v151, v63, v71\n\t"
    "v_cvt_pk_bf16_f32 v162, v148, v149\n\t"
    "v_mfma_f32_16x16x32_bf16 v[12:15], v[252:255], a[188:191], v[12:15]\n\t"
    "v_cvt_pk_bf16_f32 v163, v150, v151\n\t"
    "v_mfma_f32_16x16x32_bf16 v[12:15], v[128:131], a[192:195], v[12:15]\n\t"
    "ds_write_b128 v195, v[160:163] offset:9664\n\t"
    "v_mfma_f32_16x16x32_bf16 v[16:19], v[244:247], a[184:187], v[16:19]\n\t"
    "v_sub_f32 v144, v56, v64\n\t"
    "v_sub_f32 v145, v57, v65\n\t"
    "v_mfma_f32_16x16x32_bf16 v[16:19], v[120:123], a[188:191], v[16:19]\n\t"
    "v_sub_f32 v146, v58, v66\n\t"
    "v_sub_f32 v147, v59, v67\n\t"
    "v_mfma_f32_16x16x32_bf16 v[16:19], v[132:135], a[192:195], v[16:19]\n\t"
    "v_cvt_pk_bf16_f32 v152, v144, v145\n\t"
    "v_cvt_pk_bf16_f32 v153, v146, v147\n\t"
    "v_mfma_f32_16x16x32_bf16 v[20:23], v[248:251], a[184:187], v[20:23]\n\t"
    "ds_read_b128 a[184:187], v189 offset:6144\n\t"
    "v_sub_f32 v148, v60, v68\n\t"
    "v_mfma_f32_16x16x32_bf16 v[20:23], v[124:127], a[188:191], v[20:23]\n\t"
    "ds_read_b128 a[188:191], v189 offset:6208\n\t"
    "v_sub_f32 v149, v61, v69\n\t"
    "v_mfma_f32_16x16x32_bf16 v[20:23], v[136:139], a[192:195], v[20:23]\n\t"
    "ds_read_b128 a[192:195], v189 offset:6272\n\t"
    "v_sub_f32 v150, v62, v70\n\t"
    "s_waitcnt lgkmcnt(10)\n\t"
    "v_mfma_f32_16x16x32_bf16 v[12:15], v[140:143], a[196:199], v[12:15]\n\t"
    "v_sub_f32 v151, v63, v71\n\t"
    "v_cvt_pk_bf16_f32 v154, v148, v149\n\t"
    "v_mfma_f32_16x16x32_bf16 v[12:15], v[172:175], a[200:203], v[12:15]\n\t"
    "v_cvt_pk_bf16_f32 v155, v150, v151\n\t"
    "v_mfma_f32_16x16x32_bf16 v[12:15], v[184:187], a[204:207], v[12:15]\n\t"
    "ds_write_b128 v195, v[152:155] offset:19264\n\t"
    "v_mfma_f32_16x16x32_bf16 v[16:19], v[164:167], a[196:199], v[16:19]\n\t"
    "v_sub_f32 v144, v112, v56\n\t"
    "v_sub_f32 v145, v113, v57\n\t"
    "v_mfma_f32_16x16x32_bf16 v[16:19], v[176:179], a[200:203], v[16:19]\n\t"
    "v_sub_f32 v146, v114, v58\n\t"
    "v_sub_f32 v147, v115, v59\n\t"
    "s_waitcnt vmcnt(6)\n\t"
    "v_mfma_f32_16x16x32_bf16 v[56:59], a[164:167], a[124:127], 0\n\t"
    "ds_write_b128 v196, v[12:15] offset:12288\n\t"
    "v_mfma_f32_16x16x32_bf16 v[56:59], a[140:143], a[220:223], v[56:59]\n\t"
    "v_cvt_pk_bf16_f32 v156, v144, v145\n\t"
    "v_cvt_pk_bf16_f32 v157, v146, v147\n\t"
    "v_mfma_f32_16x16x32_bf16 v[56:59], a[144:147], a[224:227], v[56:59]\n\t"
    "v_sub_f32 v148, v116, v60\n\t"
    "v_sub_f32 v149, v117, v61\n\t"
    "v_mfma_f32_16x16x32_bf16 v[56:59], a[148:151], a[228:231], v[56:59]\n\t"
    "v_sub_f32 v150, v118, v62\n\t"
    "v_sub_f32 v151, v119, v63\n\t"
    "v_mfma_f32_16x16x32_bf16 v[84:87], a[166:169], a[136:139], 0\n\t"
    "v_cvt_pk_bf16_f32 v158, v148, v149\n\t"
    "v_cvt_pk_bf16_f32 v159, v150, v151\n\t"
    "s_waitcnt lgkmcnt(11)\n\t"
    "v_mfma_f32_16x16x32_bf16 v[84:87], a[152:155], a[208:211], v[84:87]\n\t"
    "ds_write_b128 v195, v[156:159] offset:28864\n\t"
    "v_mfma_f32_16x16x32_bf16 v[84:87], a[156:159], a[212:215], v[84:87]\n\t"
    "v_sub_f32 v144, v96, v80\n\t"
    "v_sub_f32 v145, v97, v81\n\t"
    "v_mfma_f32_16x16x32_bf16 v[84:87], a[160:163], a[216:219], v[84:87]\n\t"
    "v_sub_f32 v146, v98, v82\n\t"
    "v_sub_f32 v147, v99, v83\n\t"
    "v_mfma_f32_16x16x32_bf16 v[60:63], a[166:169], a[124:127], 0\n\t"
    "v_cvt_pk_bf16_f32 v160, v144, v145\n\t"
    "v_cvt_pk_bf16_f32 v161, v146, v147\n\t"
    "v_mfma_f32_16x16x32_bf16 v[60:63], a[152:155], a[220:223], v[60:63]\n\t"
    "v_mfma_f32_16x16x32_bf16 v[60:63], a[156:159], a[224:227], v[60:63]\n\t"
    "v_mfma_f32_16x16x32_bf16 v[60:63], a[160:163], a[228:231], v[60:63]\n\t"
    "v_sub_f32 v148, v100, v84\n\t"
    "v_sub_f32 v149, v101, v85\n\t"
    "v_mfma_f32_16x16x32_bf16 v[16:19], v[48:51], a[204:207], v[16:19]\n\t"
    "v_sub_f32 v150, v102, v86\n\t"
    "v_sub_f32 v151, v103, v87\n\t"
    "v_mfma_f32_16x16x32_bf16 v[20:23], v[168:171], a[196:199], v[20:23]\n\t"
    "ds_read_b128 a[196:199], v190 offset:6144\n\t"
    "v_cvt_pk_bf16_f32 v162, v148, v149\n\t"
    "v_mfma_f32_16x16x32_bf16 v[20:23], v[180:183], a[200:203], v[20:23]\n\t"
    "ds_read_b128 a[200:203], v190 offset:6208\n\t"
    "v_cvt_pk_bf16_f32 v163, v150, v151\n\t"
    "v_mfma_f32_16x16x32_bf16 v[20:23], v[52:55], a[204:207], v[20:23]\n\t"
    "ds_write_b128 v196, v[16:19] offset:13312\n\t"
    "s_waitcnt lgkmcnt(10)\n\t"
    "v_mfma_f32_16x16x32_bf16 v[24:27], a[108:111], a[172:175], 0\n\t"
    "v_mfma_f32_16x16x32_bf16 v[24:27], v[216:219], a[176:179], v[24:27]\n\t"
    "v_mfma_f32_16x16x32_bf16 v[24:27], v[228:231], a[180:183], v[24:27]\n\t"
    "ds_write_b128 v195, v[160:163] offset:128\n\t"
    "v_mfma_f32_16x16x32_bf16 v[28:31], a[112:115], a[172:175], 0\n\t"
    "v_add_f32 v144, v72, v80\n\t"
    "v_add_f32 v145, v73, v81\n\t"
    "v_mfma_f32_16x16x32_bf16 v[28:31], v[220:223], a[176:179], v[28:31]\n\t"
    "v_add_f32 v146, v74, v82\n\t"
    "v_add_f32 v147, v75, v83\n\t"
    "v_mfma_f32_16x16x32_bf16 v[28:31], v[232:235], a[180:183], v[28:31]\n\t"
    "ds_write_b128 v196, v[20:23] offset:14336\n\t"
    "v_mfma_f32_16x16x32_bf16 v[32:35], v[212:215], a[172:175], 0\n\t"
    "v_cvt_pk_bf16_f32 v152, v144, v145\n\t"
    "v_cvt_pk_bf16_f32 v153, v146, v147\n\t"
    "v_mfma_f32_16x16x32_bf16 v[32:35], v[224:227], a[176:179], v[32:35]\n\t"
    "v_add_f32 v148, v76, v84\n\t"
    "v_add_f32 v149, v77, v85\n\t"
    "v_mfma_f32_16x16x32_bf16 v[32:35], v[236:239], a[180:183], v[32:35]\n\t"
    "ds_read_b128 a[204:207], v190 offset:6272\n\t"
    "v_add_f32 v150, v78, v86\n\t"
    "s_waitcnt lgkmcnt(9)\n\t"
    "v_mfma_f32_16x16x32_bf16 v[24:27], v[240:243], a[184:187], v[24:27]\n\t"
    "ds_read_b128 a[172:175], v191 offset:0\n\t"
    "v_add_f32 v151, v79, v87\n\t"
    "v_mfma_f32_16x16x32_bf16 v[24:27], v[252:255], a[188:191], v[24:27]\n\t"
    "ds_read_b128 a[176:179], v191 offset:64\n\t"
    "v_cvt_pk_bf16_f32 v154, v148, v149\n\t"
    "v_mfma_f32_16x16x32_bf16 v[24:27], v[128:131], a[192:195], v[24:27]\n\t"
    "ds_read_b128 a[180:183], v191 offset:128\n\t"
    "v_cvt_pk_bf16_f32 v155, v150, v151\n\t"
    "v_mfma_f32_16x16x32_bf16 v[28:31], v[244:247], a[184:187], v[28:31]\n\t"
    "ds_write_b128 v195, v[152:155] offset:9728\n\t"
    "v_mfma_f32_16x16x32_bf16 v[28:31], v[120:123], a[188:191], v[28:31]\n\t"
    "v_sub_f32 v144, v72, v80\n\t"
    "v_sub_f32 v145, v73, v81\n\t"
    "v_mfma_f32_16x16x32_bf16 v[28:31], v[132:135], a[192:195], v[28:31]\n\t"
    "v_sub_f32 v146, v74, v82\n\t"
    "v_sub_f32 v147, v75, v83\n\t"
    "v_mfma_f32_16x16x32_bf16 v[32:35], v[248:251], a[184:187], v[32:35]\n\t"
    "ds_read_b128 a[184:187], v192 offset:0\n\t"
    "v_cvt_pk_bf16_f32 v156, v144, v145\n\t"
    "v_mfma_f32_16x16x32_bf16 v[32:35], v[124:127], a[188:191], v[32:35]\n\t"
    "ds_read_b128 a[188:191], v192 offset:64\n\t"
    "v_cvt_pk_bf16_f32 v157, v146, v147\n\t"
    "v_mfma_f32_16x16x32_bf16 v[32:35], v[136:139], a[192:195], v[32:35]\n\t"
    "ds_read_b128 a[192:195], v192 offset:128\n\t"
    "v_sub_f32 v148, v76, v84\n\t"
    "v_sub_f32 v149, v77, v85\n\t"
    "v_sub_f32 v150, v78, v86\n\t"
    "v_sub_f32 v151, v79, v87\n\t"
    "v_cvt_pk_bf16_f32 v158, v148, v149\n\t"
    "s_waitcnt lgkmcnt(7)\n\t"
    "v_mfma_f32_16x16x32_bf16 v[24:27], v[140:143], a[196:199], v[24:27]\n\t"
    "v_cvt_pk_bf16_f32 v159, v150, v151\n\t"
    "v_mfma_f32_16x16x32_bf16 v[24:27], v[172:175], a[200:203], v[24:27]\n\t"
    "ds_write_b128 v195, v[156:159] offset:19328\n\t"
    "v_mfma_f32_16x16x32_bf16 v[24:27], v[184:187], a[204:207], v[24:27]\n\t"
    "v_sub_f32 v144, v56, v72\n\t"
    "v_sub_f32 v145, v57, v73\n\t"
    "v_mfma_f32_16x16x32_bf16 v[28:31], v[164:167], a[196:199], v[28:31]\n\t"
    "v_sub_f32 v146, v58, v74\n\t"
    "v_sub_f32 v147, v59, v75\n\t"
    "v_mfma_f32_16x16x32_bf16 v[28:31], v[176:179], a[200:203], v[28:31]\n\t"
    "v_cvt_pk_bf16_f32 v160, v144, v145\n\t"
    "v_cvt_pk_bf16_f32 v161, v146, v147\n\t"
    "v_mfma_f32_16x16x32_bf16 v[28:31], v[48:51], a[204:207], v[28:31]\n\t"
    "ds_write_b128 v196, v[24:27] offset:24576\n\t"
    "v_mfma_f32_16x16x32_bf16 v[32:35], v[168:171], a[196:199], v[32:35]\n\t"
    "v_sub_f32 v148, v60, v76\n\t"
    "v_sub_f32 v149, v61, v77\n\t"
    "v_mfma_f32_16x16x32_bf16 v[32:35], v[180:183], a[200:203], v[32:35]\n\t"
    "v_sub_f32 v150, v62, v78\n\t"
    "v_sub_f32 v151, v63, v79\n\t"
    "v_mfma_f32_16x16x32_bf16 v[32:35], v[52:55], a[204:207], v[32:35]\n\t"
    "ds_write_b128 v196, v[28:31] offset:25600\n\t"
    "s_waitcnt lgkmcnt(7)\n\t"
    "v_mfma_f32_16x16x32_bf16 v[0:3], a[0:3], a[172:175], v[0:3]\n\t"
    "v_cvt_pk_bf16_f32 v162, v148, v149\n\t"
    "v_cvt_pk_bf16_f32 v163, v150, v151\n\t"
    "v_mfma_f32_16x16x32_bf16 v[0:3], a[12:15], a[176:179], v[0:3]\n\t"
    "v_mfma_f32_16x16x32_bf16 v[0:3], a[24:27], a[180:183], v[0:3]\n\t"
    "ds_write_b128 v195, v[160:163] offset:28928\n\t"
    "v_mfma_f32_16x16x32_bf16 v[4:7], a[4:7], a[172:175], v[4:7]\n\t"
    "v_mfma_f32_16x16x32_bf16 v[4:7], a[16:19], a[176:179], v[4:7]\n\t"
    "v_mfma_f32_16x16x32_bf16 v[4:7], a[28:31], a[180:183], v[4:7]\n\t"
    "ds_write_b128 v196, v[32:35] offset:26624\n\t"
    "v_mfma_f32_16x16x32_bf16 v[8:11], a[8:11], a[172:175], v[8:11]\n\t"
    "v_mfma_f32_16x16x32_bf16 v[8:11], a[20:23], a[176:179], v[8:11]\n\t"
    "v_mfma_f32_16x16x32_bf16 v[8:11], a[32:35], a[180:183], v[8:11]\n\t"
    "ds_read_b128 a[196:199], v193 offset:0\n\t"
    "ds_read_b128 a[200:203], v193 offset:64\n\t"
    "ds_read_b128 a[204:207], v193 offset:128\n\t"
    "ds_read_b128 a[172:175], v191 offset:3072\n\t"
    "ds_read_b128 a[176:179], v191 offset:3136\n\t"
    "ds_read_b128 a[180:183], v191 offset:3200\n\t"
    "s_waitcnt lgkmcnt(6)\n\t"
    "s_barrier\n\t"
    "v_mfma_f32_16x16x32_bf16 v[0:3], a[36:39], a[184:187], v[0:3]\n\t"
    "ds_read_b128 v[36:39], v197 offset:0\n\t"
    "v_mfma_f32_16x16x32_bf16 v[0:3], a[48:51], a[188:191], v[0:3]\n\t"
    "ds_read_b128 v[40:43], v197 offset:1024\n\t"
    "v_mfma_f32_16x16x32_bf16 v[0:3], a[60:63], a[192:195], v[0:3]\n\t"
    "ds_read_b128 v[44:47], v197 offset:2048\n\t"
    "s_add_u32 s76, s74, 0x90000\n\t"
    "v_mfma_f32_16x16x32_bf16 v[4:7], a[40:43], a[184:187], v[4:7]\n\t"
    "ds_read_b64 a[170:171], v211 offset:8192\n\t"
    "s_add_u32 s86, s74, 0x6cc00\n\t"
    "v_mfma_f32_16x16x32_bf16 v[4:7], a[52:55], a[188:191], v[4:7]\n\t"
    "v_mfma_f32_16x16x32_bf16 v[4:7], a[64:67], a[192:195], v[4:7]\n\t"
    "v_mfma_f32_16x16x32_bf16 v[8:11], a[44:47], a[184:187], v[8:11]\n\t"
    "ds_read_b128 a[184:187], v192 offset:3072\n\t"
    "v_mfma_f32_16x16x32_bf16 v[8:11], a[56:59], a[188:191], v[8:11]\n\t"
    "ds_read_b128 a[188:191], v192 offset:3136\n\t"
    "v_mfma_f32_16x16x32_bf16 v[8:11], a[68:71], a[192:195], v[8:11]\n\t"
    "ds_read_b128 a[192:195], v192 offset:3200\n\t"
    "s_waitcnt lgkmcnt(10)\n\t"
    "v_mfma_f32_16x16x32_bf16 v[0:3], a[72:75], a[196:199], v[0:3]\n\t"
    "v_mfma_f32_16x16x32_bf16 v[0:3], a[84:87], a[200:203], v[0:3]\n\t"
    "v_mfma_f32_16x16x32_bf16 v[0:3], a[96:99], a[204:207], v[0:3]\n\t"
    "v_mfma_f32_16x16x32_bf16 v[4:7], a[76:79], a[196:199], v[4:7]\n\t"
    "v_mfma_f32_16x16x32_bf16 v[4:7], a[88:91], a[200:203], v[4:7]\n\t"
    "v_mfma_f32_16x16x32_bf16 v[4:7], a[100:103], a[204:207], v[4:7]\n\t"
    "v_mfma_f32_16x16x32_bf16 v[8:11], a[80:83], a[196:199], v[8:11]\n\t"
    "s_waitcnt lgkmcnt(3)\n\t"
    "buffer_store_dwordx2 a[170:171], v201, s[52:55], s86 offen offset:0 sc1\n\t"
    "v_mfma_f32_16x16x32_bf16 v[8:11], a[92:95], a[200:203], v[8:11]\n\t"
    "ds_read_b128 a[196:199], v193 offset:3072\n\t"
    "v_mfma_f32_16x16x32_bf16 v[8:11], a[104:107], a[204:207], v[8:11]\n\t"
    "ds_read_b128 a[200:203], v193 offset:3136\n\t"
    "v_fma_f32 v36, v36, s75, v0\n\t"
    "v_mfma_f32_16x16x32_bf16 v[12:15], a[0:3], a[172:175], v[12:15]\n\t"
    "ds_read_b128 a[204:207], v193 offset:3200\n\t"
    "v_fma_f32 v37, v37, s75, v1\n\t"
    "v_mfma_f32_16x16x32_bf16 v[12:15], a[12:15], a[176:179], v[12:15]\n\t"
    "v_fma_f32 v38, v38, s75, v2\n\t"
    "v_fma_f32 v39, v39, s75, v3\n\t"
    "v_mfma_f32_16x16x32_bf16 v[12:15], a[24:27], a[180:183], v[12:15]\n\t"
    "v_cvt_pk_bf16_f32 v36, v36, v37\n\t"
    "v_cvt_pk_bf16_f32 v37, v38, v39\n\t"
    "v_mfma_f32_16x16x32_bf16 v[16:19], a[4:7], a[172:175], v[16:19]\n\t"
    "v_fma_f32 v40, v40, s75, v4\n\t"
    "v_fma_f32 v41, v41, s75, v5\n\t"
    "v_mfma_f32_16x16x32_bf16 v[16:19], a[16:19], a[176:179], v[16:19]\n\t"
    "v_fma_f32 v42, v42, s75, v6\n\t"
    "v_fma_f32 v43, v43, s75, v7\n\t"
    "v_mfma_f32_16x16x32_bf16 v[16:19], a[28:31], a[180:183], v[16:19]\n\t"
    "v_cvt_pk_bf16_f32 v38, v40, v41\n\t"
    "v_cvt_pk_bf16_f32 v39, v42, v43\n\t"
    "v_mfma_f32_16x16x32_bf16 v[20:23], a[8:11], a[172:175], v[20:23]\n\t"
    "buffer_store_dwordx4 v[36:39], v200, s[52:55], s76 offen offset:0 sc1\n\t"
    "v_mfma_f32_16x16x32_bf16 v[20:23], a[20:23], a[176:179], v[20:23]\n\t"
    "ds_read_b128 a[172:175], v191 offset:6144\n\t"
    "v_fma_f32 v44, v44, s75, v8\n\t"
    "v_mfma_f32_16x16x32_bf16 v[20:23], a[32:35], a[180:183], v[20:23]\n\t"
    "ds_read_b128 a[176:179], v191 offset:6208\n\t"
    "v_fma_f32 v45, v45, s75, v9\n\t"
    "s_waitcnt lgkmcnt(5)\n\t"
    "v_mfma_f32_16x16x32_bf16 v[12:15], a[36:39], a[184:187], v[12:15]\n\t"
    "ds_read_b128 a[180:183], v191 offset:6272\n\t"
    "v_fma_f32 v46, v46, s75, v10\n\t"
    "v_mfma_f32_16x16x32_bf16 v[12:15], a[48:51], a[188:191], v[12:15]\n\t"
    "v_fma_f32 v47, v47, s75, v11\n\t"
    "v_cvt_pk_bf16_f32 v44, v44, v45\n\t"
    "v_mfma_f32_16x16x32_bf16 v[12:15], a[60:63], a[192:195], v[12:15]\n\t"
    "v_cvt_pk_bf16_f32 v45, v46, v47\n\t"
    "ds_read_b64 a[170:171], v211 offset:10240\n\t"
    "s_add_u32 s86, s74, 0x6d800\n\t"
    "v_mfma_f32_16x16x32_bf16 v[16:19], a[40:43], a[184:187], v[16:19]\n\t"
    "ds_write_b64 v210, v[44:45] offset:0\n\t"
    "v_mfma_f32_16x16x32_bf16 v[16:19], a[52:55], a[188:191], v[16:19]\n\t"
    "v_mfma_f32_16x16x32_bf16 v[16:19], a[64:67], a[192:195], v[16:19]\n\t"
    "ds_read_b128 v[36:39], v197 offset:12288\n\t"
    "v_mfma_f32_16x16x32_bf16 v[20:23], a[44:47], a[184:187], v[20:23]\n\t"
    "ds_read_b128 a[184:187], v192 offset:6144\n\t"
    "v_mfma_f32_16x16x32_bf16 v[20:23], a[56:59], a[188:191], v[20:23]\n\t"
    "ds_read_b128 a[188:191], v192 offset:6208\n\t"
    "v_mfma_f32_16x16x32_bf16 v[20:23], a[68:71], a[192:195], v[20:23]\n\t"
    "ds_read_b128 a[192:195], v192 offset:6272\n\t"
    "s_waitcnt lgkmcnt(9)\n\t"
    "v_mfma_f32_16x16x32_bf16 v[12:15], a[72:75], a[196:199], v[12:15]\n\t"
    "ds_read_b128 v[40:43], v197 offset:13312\n\t"
    "v_mfma_f32_16x16x32_bf16 v[12:15], a[84:87], a[200:203], v[12:15]\n\t"
    "ds_read_b128 v[44:47], v197 offset:14336\n\t"
    "s_add_u32 s77, s74, 0x90c00\n\t"
    "v_mfma_f32_16x16x32_bf16 v[12:15], a[96:99], a[204:207], v[12:15]\n\t"
    "v_mfma_f32_16x16x32_bf16 v[16:19], a[76:79], a[196:199], v[16:19]\n\t"
    "v_mfma_f32_16x16x32_bf16 v[16:19], a[88:91], a[200:203], v[16:19]\n\t"
    "v_mfma_f32_16x16x32_bf16 v[16:19], a[100:103], a[204:207], v[16:19]\n\t"
    "s_waitcnt lgkmcnt(7)\n\t"
    "buffer_store_dwordx2 a[170:171], v201, s[52:55], s86 offen offset:0 sc1\n\t"
    "v_mfma_f32_16x16x32_bf16 v[20:23], a[80:83], a[196:199], v[20:23]\n\t"
    "ds_read_b128 a[196:199], v193 offset:6144\n\t"
    "v_mfma_f32_16x16x32_bf16 v[20:23], a[92:95], a[200:203], v[20:23]\n\t"
    "ds_read_b128 a[200:203], v193 offset:6208\n\t"
    "v_mfma_f32_16x16x32_bf16 v[20:23], a[104:107], a[204:207], v[20:23]\n\t"
    "ds_read_b128 a[204:207], v193 offset:6272\n\t"
    "v_mfma_f32_16x16x32_bf16 v[24:27], a[0:3], a[172:175], v[24:27]\n\t"
    "v_mfma_f32_16x16x32_bf16 v[24:27], a[12:15], a[176:179], v[24:27]\n\t"
    "v_mfma_f32_16x16x32_bf16 v[24:27], a[24:27], a[180:183], v[24:27]\n\t"
    "v_mfma_f32_16x16x32_bf16 v[28:31], a[4:7], a[172:175], v[28:31]\n\t"
    "s_waitcnt lgkmcnt(3)\n\t"
    "v_fma_f32 v36, v36, s75, v12\n\t"
    "v_mfma_f32_16x16x32_bf16 v[28:31], a[16:19], a[176:179], v[28:31]\n\t"
    "v_fma_f32 v37, v37, s75, v13\n\t"
    "v_fma_f32 v38, v38, s75, v14\n\t"
    "v_mfma_f32_16x16x32_bf16 v[28:31], a[28:31], a[180:183], v[28:31]\n\t"
    "v_fma_f32 v39, v39, s75, v15\n\t"
    "v_cvt_pk_bf16_f32 v36, v36, v37\n\t"
    "v_mfma_f32_16x16x32_bf16 v[32:35], a[8:11], a[172:175], v[32:35]\n\t"
    "v_cvt_pk_bf16_f32 v37, v38, v39\n\t"
    "v_fma_f32 v40, v40, s75, v16\n\t"
    "v_mfma_f32_16x16x32_bf16 v[32:35], a[20:23], a[176:179], v[32:35]\n\t"
    "v_fma_f32 v41, v41, s75, v17\n\t"
    "v_fma_f32 v42, v42, s75, v18\n\t"
    "v_mfma_f32_16x16x32_bf16 v[32:35], a[32:35], a[180:183], v[32:35]\n\t"
    "v_fma_f32 v43, v43, s75, v19\n\t"
    "v_cvt_pk_bf16_f32 v38, v40, v41\n\t"
    "v_cvt_pk_bf16_f32 v39, v42, v43\n\t"
    "buffer_store_dwordx4 v[36:39], v200, s[52:55], s77 offen offset:0 sc1\n\t"
    "v_fma_f32 v44, v44, s75, v20\n\t"
    "s_waitcnt lgkmcnt(0)\n\t"
    "s_barrier\n\t"
    "v_mfma_f32_16x16x32_bf16 v[24:27], a[36:39], a[184:187], v[24:27]\n\t"
    "ds_read_b128 a[172:175], v188 offset:38400\n\t"
    "v_fma_f32 v45, v45, s75, v21\n\t"
    "v_mfma_f32_16x16x32_bf16 v[24:27], a[48:51], a[188:191], v[24:27]\n\t"
    "ds_read_b128 a[176:179], v188 offset:38464\n\t"
    "v_fma_f32 v46, v46, s75, v22\n\t"
    "v_mfma_f32_16x16x32_bf16 v[24:27], a[60:63], a[192:195], v[24:27]\n\t"
    "ds_read_b128 a[180:183], v188 offset:38528\n\t"
    "v_fma_f32 v47, v47, s75, v23\n\t"
    "v_mfma_f32_16x16x32_bf16 v[28:31], a[40:43], a[184:187], v[28:31]\n\t"
    "v_cvt_pk_bf16_f32 v44, v44, v45\n\t"
    "v_cvt_pk_bf16_f32 v45, v46, v47\n\t"
    "v_mfma_f32_16x16x32_bf16 v[28:31], a[52:55], a[188:191], v[28:31]\n\t"
    "ds_write_b64 v210, v[44:45] offset:2048\n\t"
    "v_mfma_f32_16x16x32_bf16 v[28:31], a[64:67], a[192:195], v[28:31]\n\t"
    "v_mfma_f32_16x16x32_bf16 v[32:35], a[44:47], a[184:187], v[32:35]\n\t"
    "ds_read_b128 a[184:187], v189 offset:38400\n\t"
    "v_mfma_f32_16x16x32_bf16 v[32:35], a[56:59], a[188:191], v[32:35]\n\t"
    "ds_read_b128 a[188:191], v189 offset:38464\n\t"
    "v_mfma_f32_16x16x32_bf16 v[32:35], a[68:71], a[192:195], v[32:35]\n\t"
    "ds_read_b128 a[192:195], v189 offset:38528\n\t"
    "v_mfma_f32_16x16x32_bf16 v[24:27], a[72:75], a[196:199], v[24:27]\n\t"
    "ds_read_b128 v[36:39], v197 offset:24576\n\t"
    "v_mfma_f32_16x16x32_bf16 v[24:27], a[84:87], a[200:203], v[24:27]\n\t"
    "ds_read_b128 v[40:43], v197 offset:25600\n\t"
    "v_mfma_f32_16x16x32_bf16 v[24:27], a[96:99], a[204:207], v[24:27]\n\t"
    "ds_read_b128 v[44:47], v197 offset:26624\n\t"
    "s_add_u32 s76, s74, 0x91800\n\t"
    "v_mfma_f32_16x16x32_bf16 v[28:31], a[76:79], a[196:199], v[28:31]\n\t"
    "ds_read_b64 a[170:171], v211 offset:0\n\t"
    "s_add_u32 s86, s74, 0x90000\n\t"
    "v_mfma_f32_16x16x32_bf16 v[28:31], a[88:91], a[200:203], v[28:31]\n\t"
    "v_mfma_f32_16x16x32_bf16 v[28:31], a[100:103], a[204:207], v[28:31]\n\t"
    "v_mfma_f32_16x16x32_bf16 v[32:35], a[80:83], a[196:199], v[32:35]\n\t"
    "ds_read_b128 a[196:199], v190 offset:38400\n\t"
    "v_mfma_f32_16x16x32_bf16 v[32:35], a[92:95], a[200:203], v[32:35]\n\t"
    "ds_read_b128 a[200:203], v190 offset:38464\n\t"
    "v_mfma_f32_16x16x32_bf16 v[32:35], a[104:107], a[204:207], v[32:35]\n\t"
    "ds_read_b128 a[204:207], v190 offset:38528\n\t"
    "s_waitcnt lgkmcnt(11)\n\t"
    "v_mfma_f32_16x16x32_bf16 v[0:3], a[108:111], a[172:175], 0\n\t"
    "v_mfma_f32_16x16x32_bf16 v[0:3], v[216:219], a[176:179], v[0:3]\n\t"
    "v_mfma_f32_16x16x32_bf16 v[0:3], v[228:231], a[180:183], v[0:3]\n\t"
    "v_mfma_f32_16x16x32_bf16 v[4:7], a[112:115], a[172:175], 0\n\t"
    "v_mfma_f32_16x16x32_bf16 v[4:7], v[220:223], a[176:179], v[4:7]\n\t"
    "v_mfma_f32_16x16x32_bf16 v[4:7], v[232:235], a[180:183], v[4:7]\n\t"
    "s_waitcnt lgkmcnt(4)\n\t"
    "v_fma_f32 v36, v36, s75, v24\n\t"
    "v_fma_f32 v37, v37, s75, v25\n\t"
    "v_mfma_f32_16x16x32_bf16 v[8:11], v[212:215], a[172:175], 0\n\t"
    "s_waitcnt lgkmcnt(3)\n\t"
    "buffer_store_dwordx2 a[170:171], v201, s[52:55], s86 offen offset:0 sc1\n\t"
    "v_mfma_f32_16x16x32_bf16 v[8:11], v[224:227], a[176:179], v[8:11]\n\t"
    "ds_read_b128 a[172:175], v188 offset:41472\n\t"
    "v_fma_f32 v38, v38, s75, v26\n\t"
    "v_mfma_f32_16x16x32_bf16 v[8:11], v[236:239], a[180:183], v[8:11]\n\t"
    "ds_read_b128 a[176:179], v188 offset:41536\n\t"
    "v_fma_f32 v39, v39, s75, v27\n\t"
    "v_mfma_f32_16x16x32_bf16 v[0:3], v[240:243], a[184:187], v[0:3]\n\t"
    "ds_read_b128 a[180:183], v188 offset:41600\n\t"
    "v_cvt_pk_bf16_f32 v36, v36, v37\n\t"
    "v_mfma_f32_16x16x32_bf16 v[0:3], v[252:255], a[188:191], v[0:3]\n\t"
    "v_cvt_pk_bf16_f32 v37, v38, v39\n\t"
    "v_fma_f32 v40, v40, s75, v28\n\t"
    "v_mfma_f32_16x16x32_bf16 v[0:3], v[128:131], a[192:195], v[0:3]\n\t"
    "v_fma_f32 v41, v41, s75, v29\n\t"
    "v_fma_f32 v42, v42, s75, v30\n\t"
    "v_mfma_f32_16x16x32_bf16 v[4:7], v[244:247], a[184:187], v[4:7]\n\t"
    "v_fma_f32 v43, v43, s75, v31\n\t"
    "v_cvt_pk_bf16_f32 v38, v40, v41\n\t"
    "v_mfma_f32_16x16x32_bf16 v[4:7], v[120:123], a[188:191], v[4:7]\n\t"
    "v_cvt_pk_bf16_f32 v39, v42, v43\n\t"
    "v_mfma_f32_16x16x32_bf16 v[4:7], v[132:135], a[192:195], v[4:7]\n\t"
    "buffer_store_dwordx4 v[36:39], v200, s[52:55], s76 offen offset:0 sc1\n\t"
    "v_mfma_f32_16x16x32_bf16 v[8:11], v[248:251], a[184:187], v[8:11]\n\t"
    "ds_read_b128 a[184:187], v189 offset:41472\n\t"
    "v_fma_f32 v44, v44, s75, v32\n\t"
    "v_mfma_f32_16x16x32_bf16 v[8:11], v[124:127], a[188:191], v[8:11]\n\t"
    "ds_read_b128 a[188:191], v189 offset:41536\n\t"
    "v_fma_f32 v45, v45, s75, v33\n\t"
    "v_mfma_f32_16x16x32_bf16 v[8:11], v[136:139], a[192:195], v[8:11]\n\t"
    "ds_read_b128 a[192:195], v189 offset:41600\n\t"
    "v_fma_f32 v46, v46, s75, v34\n\t"
    "s_waitcnt lgkmcnt(6)\n\t"
    "v_mfma_f32_16x16x32_bf16 v[0:3], v[140:143], a[196:199], v[0:3]\n\t"
    "v_fma_f32 v47, v47, s75, v35\n\t"
    "v_cvt_pk_bf16_f32 v44, v44, v45\n\t"
    "v_mfma_f32_16x16x32_bf16 v[0:3], v[172:175], a[200:203], v[0:3]\n\t"
    "v_cvt_pk_bf16_f32 v45, v46, v47\n\t"
    "v_mfma_f32_16x16x32_bf16 v[0:3], v[184:187], a[204:207], v[0:3]\n\t"
    "ds_write_b64 v210, v[44:45] offset:4096\n\t"
    "v_mfma_f32_16x16x32_bf16 v[4:7], v[164:167], a[196:199], v[4:7]\n\t"
    "v_mfma_f32_16x16x32_bf16 v[4:7], v[176:179], a[200:203], v[4:7]\n\t"
    "v_mfma_f32_16x16x32_bf16 v[4:7], v[48:51], a[204:207], v[4:7]\n\t"
    "v_mfma_f32_16x16x32_bf16 v[8:11], v[168:171], a[196:199], v[8:11]\n\t"
    "ds_read_b128 a[196:199], v190 offset:41472\n\t"
    "v_mfma_f32_16x16x32_bf16 v[8:11], v[180:183], a[200:203], v[8:11]\n\t"
    "ds_read_b128 a[200:203], v190 offset:41536\n\t"
    "v_mfma_f32_16x16x32_bf16 v[8:11], v[52:55], a[204:207], v[8:11]\n\t"
    "ds_write_b128 v196, v[0:3] offset:36864\n\t"
    "s_waitcnt lgkmcnt(7)\n\t"
    "v_mfma_f32_16x16x32_bf16 v[12:15], a[108:111], a[172:175], 0\n\t"
    "v_mfma_f32_16x16x32_bf16 v[12:15], v[216:219], a[176:179], v[12:15]\n\t"
    "v_mfma_f32_16x16x32_bf16 v[12:15], v[228:231], a[180:183], v[12:15]\n\t"
    "ds_write_b128 v196, v[4:7] offset:37888\n\t"
    "v_mfma_f32_16x16x32_bf16 v[16:19], a[112:115], a[172:175], 0\n\t"
    "v_mfma_f32_16x16x32_bf16 v[16:19], v[220:223], a[176:179], v[16:19]\n\t"
    "v_mfma_f32_16x16x32_bf16 v[16:19], v[232:235], a[180:183], v[16:19]\n\t"
    "ds_write_b128 v196, v[8:11] offset:38912\n\t"
    "v_mfma_f32_16x16x32_bf16 v[20:23], v[212:215], a[172:175], 0\n\t"
    "v_mfma_f32_16x16x32_bf16 v[20:23], v[224:227], a[176:179], v[20:23]\n\t"
    "v_mfma_f32_16x16x32_bf16 v[20:23], v[236:239], a[180:183], v[20:23]\n\t"
    "ds_read_b128 a[204:207], v190 offset:41600\n\t"
    "s_waitcnt lgkmcnt(7)\n\t"
    "v_mfma_f32_16x16x32_bf16 v[12:15], v[240:243], a[184:187], v[12:15]\n\t"
    "ds_read_b128 a[172:175], v188 offset:44544\n\t"
    "v_mfma_f32_16x16x32_bf16 v[12:15], v[252:255], a[188:191], v[12:15]\n\t"
    "ds_read_b128 a[176:179], v188 offset:44608\n\t"
    "v_mfma_f32_16x16x32_bf16 v[12:15], v[128:131], a[192:195], v[12:15]\n\t"
    "ds_read_b128 a[180:183], v188 offset:44672\n\t"
    "v_mfma_f32_16x16x32_bf16 v[16:19], v[244:247], a[184:187], v[16:19]\n\t"
    "v_mfma_f32_16x16x32_bf16 v[16:19], v[120:123], a[188:191], v[16:19]\n\t"
    "v_mfma_f32_16x16x32_bf16 v[16:19], v[132:135], a[192:195], v[16:19]\n\t"
    "v_mfma_f32_16x16x32_bf16 v[20:23], v[248:251], a[184:187], v[20:23]\n\t"
    "ds_read_b128 a[184:187], v189 offset:44544\n\t"
    "v_mfma_f32_16x16x32_bf16 v[20:23], v[124:127], a[188:191], v[20:23]\n\t"
    "ds_read_b128 a[188:191], v189 offset:44608\n\t"
    "v_mfma_f32_16x16x32_bf16 v[20:23], v[136:139], a[192:195], v[20:23]\n\t"
    "ds_read_b128 a[192:195], v189 offset:44672\n\t"
    "s_waitcnt lgkmcnt(6)\n\t"
    "v_mfma_f32_16x16x32_bf16 v[12:15], v[140:143], a[196:199], v[12:15]\n\t"
    "v_mfma_f32_16x16x32_bf16 v[12:15], v[172:175], a[200:203], v[12:15]\n\t"
    "v_mfma_f32_16x16x32_bf16 v[12:15], v[184:187], a[204:207], v[12:15]\n\t"
    "v_mfma_f32_16x16x32_bf16 v[16:19], v[164:167], a[196:199], v[16:19]\n\t"
    "v_mfma_f32_16x16x32_bf16 v[16:19], v[176:179], a[200:203], v[16:19]\n\t"
    "v_mfma_f32_16x16x32_bf16 v[16:19], v[48:51], a[204:207], v[16:19]\n\t"
    "v_mfma_f32_16x16x32_bf16 v[20:23], v[168:171], a[196:199], v[20:23]\n\t"
    "ds_read_b128 a[196:199], v190 offset:44544\n\t"
    "v_mfma_f32_16x16x32_bf16 v[20:23], v[180:183], a[200:203], v[20:23]\n\t"
    "ds_read_b128 a[200:203], v190 offset:44608\n\t"
    "v_mfma_f32_16x16x32_bf16 v[20:23], v[52:55], a[204:207], v[20:23]\n\t"
    "ds_write_b128 v196, v[12:15] offset:0\n\t"
    "s_waitcnt lgkmcnt(6)\n\t"
    "v_mfma_f32_16x16x32_bf16 v[24:27], a[108:111], a[172:175], 0\n\t"
    "v_mfma_f32_16x16x32_bf16 v[24:27], v[216:219], a[176:179], v[24:27]\n\t"
    "v_mfma_f32_16x16x32_bf16 v[24:27], v[228:231], a[180:183], v[24:27]\n\t"
    "ds_write_b128 v196, v[16:19] offset:1024\n\t"
    "v_mfma_f32_16x16x32_bf16 v[28:31], a[112:115], a[172:175], 0\n\t"
    "v_mfma_f32_16x16x32_bf16 v[28:31], v[220:223], a[176:179], v[28:31]\n\t"
    "v_mfma_f32_16x16x32_bf16 v[28:31], v[232:235], a[180:183], v[28:31]\n\t"
    "ds_write_b128 v196, v[20:23] offset:2048\n\t"
    "v_mfma_f32_16x16x32_bf16 v[32:35], v[212:215], a[172:175], 0\n\t"
    "v_mfma_f32_16x16x32_bf16 v[32:35], v[224:227], a[176:179], v[32:35]\n\t"
    "v_mfma_f32_16x16x32_bf16 v[32:35], v[236:239], a[180:183], v[32:35]\n\t"
    "ds_read_b128 a[204:207], v190 offset:44672\n\t"
    "s_waitcnt lgkmcnt(6)\n\t"
    "v_mfma_f32_16x16x32_bf16 v[24:27], v[240:243], a[184:187], v[24:27]\n\t"
    "ds_read_b128 a[172:175], v191 offset:38400\n\t"
    "v_mfma_f32_16x16x32_bf16 v[24:27], v[252:255], a[188:191], v[24:27]\n\t"
    "ds_read_b128 a[176:179], v191 offset:38464\n\t"
    "v_mfma_f32_16x16x32_bf16 v[24:27], v[128:131], a[192:195], v[24:27]\n\t"
    "ds_read_b128 a[180:183], v191 offset:38528\n\t"
    "v_mfma_f32_16x16x32_bf16 v[28:31], v[244:247], a[184:187], v[28:31]\n\t"
    "v_mfma_f32_16x16x32_bf16 v[28:31], v[120:123], a[188:191], v[28:31]\n\t"
    "v_mfma_f32_16x16x32_bf16 v[28:31], v[132:135], a[192:195], v[28:31]\n\t"
    "v_mfma_f32_16x16x32_bf16 v[32:35], v[248:251], a[184:187], v[32:35]\n\t"
    "ds_read_b128 a[184:187], v192 offset:38400\n\t"
    "v_mfma_f32_16x16x32_bf16 v[32:35], v[124:127], a[188:191], v[32:35]\n\t"
    "ds_read_b128 a[188:191], v192 offset:38464\n\t"
    "v_mfma_f32_16x16x32_bf16 v[32:35], v[136:139], a[192:195], v[32:35]\n\t"
    "ds_read_b128 a[192:195], v192 offset:38528\n\t"
    "s_waitcnt lgkmcnt(6)\n\t"
    "v_mfma_f32_16x16x32_bf16 v[24:27], v[140:143], a[196:199], v[24:27]\n\t"
    "v_mfma_f32_16x16x32_bf16 v[24:27], v[172:175], a[200:203], v[24:27]\n\t"
    "v_mfma_f32_16x16x32_bf16 v[24:27], v[184:187], a[204:207], v[24:27]\n\t"
    "v_mfma_f32_16x16x32_bf16 v[28:31], v[164:167], a[196:199], v[28:31]\n\t"
    "v_mfma_f32_16x16x32_bf16 v[28:31], v[176:179], a[200:203], v[28:31]\n\t"
    "v_mfma_f32_16x16x32_bf16 v[28:31], v[48:51], a[204:207], v[28:31]\n\t"
    "v_mfma_f32_16x16x32_bf16 v[32:35], v[168:171], a[196:199], v[32:35]\n\t"
    "ds_read_b128 a[196:199], v193 offset:38400\n\t"
    "v_mfma_f32_16x16x32_bf16 v[32:35], v[180:183], a[200:203], v[32:35]\n\t"
    "ds_read_b128 a[200:203], v193 offset:38464\n\t"
    "v_mfma_f32_16x16x32_bf16 v[32:35], v[52:55], a[204:207], v[32:35]\n\t"
    "ds_write_b128 v196, v[24:27] offset:12288\n\t"
    "s_waitcnt lgkmcnt(6)\n\t"
    "v_mfma_f32_16x16x32_bf16 v[0:3], a[0:3], a[172:175], v[0:3]\n\t"
    "v_mfma_f32_16x16x32_bf16 v[0:3], a[12:15], a[176:179], v[0:3]\n\t"
    "v_mfma_f32_16x16x32_bf16 v[0:3], a[24:27], a[180:183], v[0:3]\n\t"
    "ds_write_b128 v196, v[28:31] offset:13312\n\t"
    "v_mfma_f32_16x16x32_bf16 v[4:7], a[4:7], a[172:175], v[4:7]\n\t"
    "v_mfma_f32_16x16x32_bf16 v[4:7], a[16:19], a[176:179], v[4:7]\n\t"
    "v_mfma_f32_16x16x32_bf16 v[4:7], a[28:31], a[180:183], v[4:7]\n\t"
    "ds_write_b128 v196, v[32:35] offset:14336\n\t"
    "v_mfma_f32_16x16x32_bf16 v[8:11], a[8:11], a[172:175], v[8:11]\n\t"
    "v_mfma_f32_16x16x32_bf16 v[8:11], a[20:23], a[176:179], v[8:11]\n\t"
    "v_mfma_f32_16x16x32_bf16 v[8:11], a[32:35], a[180:183], v[8:11]\n\t"
    "ds_read_b128 a[204:207], v193 offset:38528\n\t"
    "ds_read_b128 a[172:175], v191 offset:41472\n\t"
    "ds_read_b128 a[176:179], v191 offset:41536\n\t"
    "ds_read_b128 a[180:183], v191 offset:41600\n\t"
    "s_waitcnt lgkmcnt(4)\n\t"
    "s_barrier\n\t"
    "v_mfma_f32_16x16x32_bf16 v[0:3], a[36:39], a[184:187], v[0:3]\n\t"
    "ds_read_b128 v[36:39], v197 offset:36864\n\t"
    "v_mfma_f32_16x16x32_bf16 v[0:3], a[48:51], a[188:191], v[0:3]\n\t"
    "ds_read_b128 v[40:43], v197 offset:37888\n\t"
    "v_mfma_f32_16x16x32_bf16 v[0:3], a[60:63], a[192:195], v[0:3]\n\t"
    "ds_read_b128 v[44:47], v197 offset:38912\n\t"
    "s_add_u32 s77, s74, 0xb4000\n\t"
    "v_mfma_f32_16x16x32_bf16 v[4:7], a[40:43], a[184:187], v[4:7]\n\t"
    "ds_read_b64 a[170:171], v211 offset:2048\n\t"
    "s_add_u32 s86, s74, 0x90c00\n\t"
    "v_mfma_f32_16x16x32_bf16 v[4:7], a[52:55], a[188:191], v[4:7]\n\t"
    "v_mfma_f32_16x16x32_bf16 v[4:7], a[64:67], a[192:195], v[4:7]\n\t"
    "v_mfma_f32_16x16x32_bf16 v[8:11], a[44:47], a[184:187], v[8:11]\n\t"
    "ds_read_b128 a[184:187], v192 offset:41472\n\t"
    "v_mfma_f32_16x16x32_bf16 v[8:11], a[56:59], a[188:191], v[8:11]\n\t"
    "ds_read_b128 a[188:191], v192 offset:41536\n\t"
    "v_mfma_f32_16x16x32_bf16 v[8:11], a[68:71], a[192:195], v[8:11]\n\t"
    "ds_read_b128 a[192:195], v192 offset:41600\n\t"
    "s_waitcnt lgkmcnt(10)\n\t"
    "v_mfma_f32_16x16x32_bf16 v[0:3], a[72:75], a[196:199], v[0:3]\n\t"
    "v_mfma_f32_16x16x32_bf16 v[0:3], a[84:87], a[200:203], v[0:3]\n\t"
    "v_mfma_f32_16x16x32_bf16 v[0:3], a[96:99], a[204:207], v[0:3]\n\t"
    "v_mfma_f32_16x16x32_bf16 v[4:7], a[76:79], a[196:199], v[4:7]\n\t"
    "v_mfma_f32_16x16x32_bf16 v[4:7], a[88:91], a[200:203], v[4:7]\n\t"
    "v_mfma_f32_16x16x32_bf16 v[4:7], a[100:103], a[204:207], v[4:7]\n\t"
    "v_mfma_f32_16x16x32_bf16 v[8:11], a[80:83], a[196:199], v[8:11]\n\t"
    "s_waitcnt lgkmcnt(3)\n\t"
    "buffer_store_dwordx2 a[170:171], v201, s[52:55], s86 offen offset:0 sc1\n\t"
    "v_mfma_f32_16x16x32_bf16 v[8:11], a[92:95], a[200:203], v[8:11]\n\t"
    "ds_read_b128 a[196:199], v193 offset:41472\n\t"
    "v_mfma_f32_16x16x32_bf16 v[8:11], a[104:107], a[204:207], v[8:11]\n\t"
    "ds_read_b128 a[200:203], v193 offset:41536\n\t"
    "v_fma_f32 v36, v36, s75, v0\n\t"
    "v_mfma_f32_16x16x32_bf16 v[12:15], a[0:3], a[172:175], v[12:15]\n\t"
    "ds_read_b128 a[204:207], v193 offset:41600\n\t"
    "v_fma_f32 v37, v37, s75, v1\n\t"
    "v_mfma_f32_16x16x32_bf16 v[12:15], a[12:15], a[176:179], v[12:15]\n\t"
    "v_fma_f32 v38, v38, s75, v2\n\t"
    "v_fma_f32 v39, v39, s75, v3\n\t"
    "v_mfma_f32_16x16x32_bf16 v[12:15], a[24:27], a[180:183], v[12:15]\n\t"
    "v_cvt_pk_bf16_f32 v36, v36, v37\n\t"
    "v_cvt_pk_bf16_f32 v37, v38, v39\n\t"
    "v_mfma_f32_16x16x32_bf16 v[16:19], a[4:7], a[172:175], v[16:19]\n\t"
    "v_fma_f32 v40, v40, s75, v4\n\t"
    "v_fma_f32 v41, v41, s75, v5\n\t"
    "v_mfma_f32_16x16x32_bf16 v[16:19], a[16:19], a[176:179], v[16:19]\n\t"
    "v_fma_f32 v42, v42, s75, v6\n\t"
    "v_fma_f32 v43, v43, s75, v7\n\t"
    "v_mfma_f32_16x16x32_bf16 v[16:19], a[28:31], a[180:183], v[16:19]\n\t"
    "v_cvt_pk_bf16_f32 v38, v40, v41\n\t"
    "v_cvt_pk_bf16_f32 v39, v42, v43\n\t"
    "v_mfma_f32_16x16x32_bf16 v[20:23], a[8:11], a[172:175], v[20:23]\n\t"
    "buffer_store_dwordx4 v[36:39], v200, s[52:55], s77 offen offset:0 sc1\n\t"
    "v_mfma_f32_16x16x32_bf16 v[20:23], a[20:23], a[176:179], v[20:23]\n\t"
    "ds_read_b128 a[172:175], v191 offset:44544\n\t"
    "v_fma_f32 v44, v44, s75, v8\n\t"
    "v_mfma_f32_16x16x32_bf16 v[20:23], a[32:35], a[180:183], v[20:23]\n\t"
    "ds_read_b128 a[176:179], v191 offset:44608\n\t"
    "v_fma_f32 v45, v45, s75, v9\n\t"
    "s_waitcnt lgkmcnt(5)\n\t"
    "v_mfma_f32_16x16x32_bf16 v[12:15], a[36:39], a[184:187], v[12:15]\n\t"
    "ds_read_b128 a[180:183], v191 offset:44672\n\t"
    "v_fma_f32 v46, v46, s75, v10\n\t"
    "v_mfma_f32_16x16x32_bf16 v[12:15], a[48:51], a[188:191], v[12:15]\n\t"
    "v_fma_f32 v47, v47, s75, v11\n\t"
    "v_cvt_pk_bf16_f32 v44, v44, v45\n\t"
    "v_mfma_f32_16x16x32_bf16 v[12:15], a[60:63], a[192:195], v[12:15]\n\t"
    "v_cvt_pk_bf16_f32 v45, v46, v47\n\t"
    "ds_read_b64 a[170:171], v211 offset:4096\n\t"
    "s_add_u32 s86, s74, 0x91800\n\t"
    "v_mfma_f32_16x16x32_bf16 v[16:19], a[40:43], a[184:187], v[16:19]\n\t"
    "ds_write_b64 v210, v[44:45] offset:6144\n\t"
    "v_mfma_f32_16x16x32_bf16 v[16:19], a[52:55], a[188:191], v[16:19]\n\t"
    "v_mfma_f32_16x16x32_bf16 v[16:19], a[64:67], a[192:195], v[16:19]\n\t"
    "ds_read_b128 v[36:39], v197 offset:0\n\t"
    "v_mfma_f32_16x16x32_bf16 v[20:23], a[44:47], a[184:187], v[20:23]\n\t"
    "ds_read_b128 a[184:187], v192 offset:44544\n\t"
    "v_mfma_f32_16x16x32_bf16 v[20:23], a[56:59], a[188:191], v[20:23]\n\t"
    "ds_read_b128 a[188:191], v192 offset:44608\n\t"
    "v_mfma_f32_16x16x32_bf16 v[20:23], a[68:71], a[192:195], v[20:23]\n\t"
    "ds_read_b128 a[192:195], v192 offset:44672\n\t"
    "s_waitcnt lgkmcnt(9)\n\t"
    "v_mfma_f32_16x16x32_bf16 v[12:15], a[72:75], a[196:199], v[12:15]\n\t"
    "ds_read_b128 v[40:43], v197 offset:1024\n\t"
    "v_mfma_f32_16x16x32_bf16 v[12:15], a[84:87], a[200:203], v[12:15]\n\t"
    "ds_read_b128 v[44:47], v197 offset:2048\n\t"
    "s_add_u32 s76, s74, 0xb4c00\n\t"
    "v_mfma_f32_16x16x32_bf16 v[12:15], a[96:99], a[204:207], v[12:15]\n\t"
    "v_mfma_f32_16x16x32_bf16 v[16:19], a[76:79], a[196:199], v[16:19]\n\t"
    "v_mfma_f32_16x16x32_bf16 v[16:19], a[88:91], a[200:203], v[16:19]\n\t"
    "v_mfma_f32_16x16x32_bf16 v[16:19], a[100:103], a[204:207], v[16:19]\n\t"
    "s_waitcnt lgkmcnt(7)\n\t"
    "buffer_store_dwordx2 a[170:171], v201, s[52:55], s86 offen offset:0 sc1\n\t"
    "v_mfma_f32_16x16x32_bf16 v[20:23], a[80:83], a[196:199], v[20:23]\n\t"
    "ds_read_b128 a[196:199], v193 offset:44544\n\t"
    "v_mfma_f32_16x16x32_bf16 v[20:23], a[92:95], a[200:203], v[20:23]\n\t"
    "ds_read_b128 a[200:203], v193 offset:44608\n\t"
    "v_mfma_f32_16x16x32_bf16 v[20:23], a[104:107], a[204:207], v[20:23]\n\t"
    "ds_read_b128 a[204:207], v193 offset:44672\n\t"
    "v_mfma_f32_16x16x32_bf16 v[24:27], a[0:3], a[172:175], v[24:27]\n\t"
    "v_mfma_f32_16x16x32_bf16 v[24:27], a[12:15], a[176:179], v[24:27]\n\t"
    "v_mfma_f32_16x16x32_bf16 v[24:27], a[24:27], a[180:183], v[24:27]\n\t"
    "v_mfma_f32_16x16x32_bf16 v[28:31], a[4:7], a[172:175], v[28:31]\n\t"
    "s_waitcnt lgkmcnt(3)\n\t"
    "v_fma_f32 v36, v36, s75, v12\n\t"
    "v_mfma_f32_16x16x32_bf16 v[28:31], a[16:19], a[176:179], v[28:31]\n\t"
    "v_fma_f32 v37, v37, s75, v13\n\t"
    "v_fma_f32 v38, v38, s75, v14\n\t"
    "v_mfma_f32_16x16x32_bf16 v[28:31], a[28:31], a[180:183], v[28:31]\n\t"
    "v_fma_f32 v39, v39, s75, v15\n\t"
    "v_cvt_pk_bf16_f32 v36, v36, v37\n\t"
    "v_mfma_f32_16x16x32_bf16 v[32:35], a[8:11], a[172:175], v[32:35]\n\t"
    "v_cvt_pk_bf16_f32 v37, v38, v39\n\t"
    "v_fma_f32 v40, v40, s75, v16\n\t"
    "v_mfma_f32_16x16x32_bf16 v[32:35], a[20:23], a[176:179], v[32:35]\n\t"
    "v_fma_f32 v41, v41, s75, v17\n\t"
    "v_fma_f32 v42, v42, s75, v18\n\t"
    "v_mfma_f32_16x16x32_bf16 v[32:35], a[32:35], a[180:183], v[32:35]\n\t"
    "v_fma_f32 v43, v43, s75, v19\n\t"
    "v_cvt_pk_bf16_f32 v38, v40, v41\n\t"
    "v_mfma_f32_16x16x32_bf16 v[24:27], a[36:39], a[184:187], v[24:27]\n\t"
    "v_cvt_pk_bf16_f32 v39, v42, v43\n\t"
    "v_mfma_f32_16x16x32_bf16 v[24:27], a[48:51], a[188:191], v[24:27]\n\t"
    "buffer_store_dwordx4 v[36:39], v200, s[52:55], s76 offen offset:0 sc1\n\t"
    "v_mfma_f32_16x16x32_bf16 v[24:27], a[60:63], a[192:195], v[24:27]\n\t"
    "v_fma_f32 v44, v44, s75, v20\n\t"
    "v_fma_f32 v45, v45, s75, v21\n\t"
    "v_mfma_f32_16x16x32_bf16 v[28:31], a[40:43], a[184:187], v[28:31]\n\t"
    "v_fma_f32 v46, v46, s75, v22\n\t"
    "v_fma_f32 v47, v47, s75, v23\n\t"
    "v_mfma_f32_16x16x32_bf16 v[28:31], a[52:55], a[188:191], v[28:31]\n\t"
    "v_cvt_pk_bf16_f32 v44, v44, v45\n\t"
    "v_cvt_pk_bf16_f32 v45, v46, v47\n\t"
    "v_mfma_f32_16x16x32_bf16 v[28:31], a[64:67], a[192:195], v[28:31]\n\t"
    "ds_write_b64 v210, v[44:45] offset:8192\n\t"
    "v_mfma_f32_16x16x32_bf16 v[32:35], a[44:47], a[184:187], v[32:35]\n\t"
    "v_mfma_f32_16x16x32_bf16 v[32:35], a[56:59], a[188:191], v[32:35]\n\t"
    "ds_read_b128 v[36:39], v197 offset:12288\n\t"
    "v_mfma_f32_16x16x32_bf16 v[32:35], a[68:71], a[192:195], v[32:35]\n\t"
    "ds_read_b128 v[40:43], v197 offset:13312\n\t"
    "s_waitcnt lgkmcnt(3)\n\t"
    "v_mfma_f32_16x16x32_bf16 v[24:27], a[72:75], a[196:199], v[24:27]\n\t"
    "ds_read_b128 v[44:47], v197 offset:14336\n\t"
    "s_add_u32 s77, s74, 0xb5800\n\t"
    "v_mfma_f32_16x16x32_bf16 v[24:27], a[84:87], a[200:203], v[24:27]\n\t"
    "v_mfma_f32_16x16x32_bf16 v[24:27], a[96:99], a[204:207], v[24:27]\n\t"
    "v_mfma_f32_16x16x32_bf16 v[28:31], a[76:79], a[196:199], v[28:31]\n\t"
    "v_mfma_f32_16x16x32_bf16 v[28:31], a[88:91], a[200:203], v[28:31]\n\t"
    "v_mfma_f32_16x16x32_bf16 v[28:31], a[100:103], a[204:207], v[28:31]\n\t"
    "v_mfma_f32_16x16x32_bf16 v[32:35], a[80:83], a[196:199], v[32:35]\n\t"
    "v_mfma_f32_16x16x32_bf16 v[32:35], a[92:95], a[200:203], v[32:35]\n\t"
    "v_mfma_f32_16x16x32_bf16 v[32:35], a[104:107], a[204:207], v[32:35]\n\t"
    "s_waitcnt lgkmcnt(0)\n\t"
    "s_nop 0\n\t"
    "v_fma_f32 v36, v36, s75, v24\n\t"
    "v_fma_f32 v37, v37, s75, v25\n\t"
    "v_fma_f32 v38, v38, s75, v26\n\t"
    "v_fma_f32 v39, v39, s75, v27\n\t"
    "v_cvt_pk_bf16_f32 v36, v36, v37\n\t"
    "v_cvt_pk_bf16_f32 v37, v38, v39\n\t"
    "v_fma_f32 v40, v40, s75, v28\n\t"
    "v_fma_f32 v41, v41, s75, v29\n\t"
    "v_fma_f32 v42, v42, s75, v30\n\t"
    "v_fma_f32 v43, v43, s75, v31\n\t"
    "v_cvt_pk_bf16_f32 v38, v40, v41\n\t"
    "v_cvt_pk_bf16_f32 v39, v42, v43\n\t"
    "buffer_store_dwordx4 v[36:39], v200, s[52:55], s77 offen offset:0 sc1\n\t"
    "v_fma_f32 v44, v44, s75, v32\n\t"
    "v_fma_f32 v45, v45, s75, v33\n\t"
    "v_fma_f32 v46, v46, s75, v34\n\t"
    "v_fma_f32 v47, v47, s75, v35\n\t"
    "v_cvt_pk_bf16_f32 v44, v44, v45\n\t"
    "v_cvt_pk_bf16_f32 v45, v46, v47\n\t"
    "ds_write_b64 v210, v[44:45] offset:10240\n\t"
    "s_waitcnt lgkmcnt(0)\n\t"
    "s_barrier\n\t"
    "ds_read_b64 a[140:141], v211 offset:6144\n\t"
    "s_add_u32 s78, s74, 0xb4000\n\t"
    "ds_read_b64 a[144:145], v211 offset:8192\n\t"
    "s_add_u32 s79, s74, 0xb4c00\n\t"
    "ds_read_b64 a[148:149], v211 offset:10240\n\t"
    "s_add_u32 s80, s74, 0xb5800\n\t"
    "s_waitcnt lgkmcnt(2)\n\t"
    "buffer_store_dwordx2 a[140:141], v201, s[52:55], s78 offen offset:0 sc1\n\t"
    "s_waitcnt lgkmcnt(1)\n\t"
    "buffer_store_dwordx2 a[144:145], v201, s[52:55], s79 offen offset:0 sc1\n\t"
    "s_waitcnt lgkmcnt(0)\n\t"
    "buffer_store_dwordx2 a[148:149], v201, s[52:55], s80 offen offset:0 sc1\n\t"
    ".LKEND%=:\n\t"
    :
    : "s"(c_fine), "s"(p_coarse), "s"(w_lat), "s"(w_out), "s"(out), "s"(bid), "s"(wid), "s"(ws), "s"(lbase)
    : "v0", "v1", "v2", "v3", "v4", "v5", "v6", "v7", "v8", "v9", "v10", "v11", "v12", "v13", "v14", "v15", "v16", "v17", "v18", "v19", "v20", "v21", "v22", "v23", "v24", "v25", "v26", "v27", "v28", "v29", "v30", "v31", "v32", "v33", "v34", "v35", "v36", "v37", "v38", "v39", "v40", "v41", "v42", "v43", "v44", "v45", "v46", "v47", "v48", "v49", "v50", "v51", "v52", "v53", "v54", "v55", "v56", "v57", "v58", "v59", "v60", "v61", "v62", "v63", "v64", "v65", "v66", "v67", "v68", "v69", "v70", "v71", "v72", "v73", "v74", "v75", "v76", "v77", "v78", "v79", "v80", "v81", "v82", "v83", "v84", "v85", "v86", "v87", "v88", "v89", "v90", "v91", "v92", "v93", "v94", "v95", "v96", "v97", "v98", "v99", "v100", "v101", "v102", "v103", "v104", "v105", "v106", "v107", "v108", "v109", "v110", "v111", "v112", "v113", "v114", "v115", "v116", "v117", "v118", "v119", "v120", "v121", "v122", "v123", "v124", "v125", "v126", "v127", "v128", "v129", "v130", "v131", "v132", "v133", "v134", "v135", "v136", "v137", "v138", "v139", "v140", "v141", "v142", "v143", "v144", "v145", "v146", "v147", "v148", "v149", "v150", "v151", "v152", "v153", "v154", "v155", "v156", "v157", "v158", "v159", "v160", "v161", "v162", "v163", "v164", "v165", "v166", "v167", "v168", "v169", "v170", "v171", "v172", "v173", "v174", "v175", "v176", "v177", "v178", "v179", "v180", "v181", "v182", "v183", "v184", "v185", "v186", "v187", "v188", "v189", "v190", "v191", "v192", "v193", "v194", "v195", "v196", "v197", "v198", "v199", "v200", "v201", "v202", "v203", "v204", "v205", "v206", "v207", "v208", "v209", "v210", "v211", "v212", "v213", "v214", "v215", "v216", "v217", "v218", "v219", "v220", "v221", "v222", "v223", "v224", "v225", "v226", "v227", "v228", "v229", "v230", "v231", "v232", "v233", "v234", "v235", "v236", "v237", "v238", "v239", "v240", "v241", "v242", "v243", "v244", "v245", "v246", "v247", "v248", "v249", "v250", "v251", "v252", "v253", "v254", "v255", "a0", "a1", "a2", "a3", "a4", "a5", "a6", "a7", "a8", "a9", "a10", "a11", "a12", "a13", "a14", "a15", "a16", "a17", "a18", "a19", "a20", "a21", "a22", "a23", "a24", "a25", "a26", "a27", "a28", "a29", "a30", "a31", "a32", "a33", "a34", "a35", "a36", "a37", "a38", "a39", "a40", "a41", "a42", "a43", "a44", "a45", "a46", "a47", "a48", "a49", "a50", "a51", "a52", "a53", "a54", "a55", "a56", "a57", "a58", "a59", "a60", "a61", "a62", "a63", "a64", "a65", "a66", "a67", "a68", "a69", "a70", "a71", "a72", "a73", "a74", "a75", "a76", "a77", "a78", "a79", "a80", "a81", "a82", "a83", "a84", "a85", "a86", "a87", "a88", "a89", "a90", "a91", "a92", "a93", "a94", "a95", "a96", "a97", "a98", "a99", "a100", "a101", "a102", "a103", "a104", "a105", "a106", "a107", "a108", "a109", "a110", "a111", "a112", "a113", "a114", "a115", "a116", "a117", "a118", "a119", "a120", "a121", "a122", "a123", "a124", "a125", "a126", "a127", "a128", "a129", "a130", "a131", "a132", "a133", "a134", "a135", "a136", "a137", "a138", "a139", "a140", "a141", "a142", "a143", "a144", "a145", "a146", "a147", "a148", "a149", "a150", "a151", "a152", "a153", "a154", "a155", "a156", "a157", "a158", "a159", "a160", "a161", "a162", "a163", "a164", "a165", "a166", "a167", "a168", "a169", "a170", "a171", "a172", "a173", "a174", "a175", "a176", "a177", "a178", "a179", "a180", "a181", "a182", "a183", "a184", "a185", "a186", "a187", "a188", "a189", "a190", "a191", "a192", "a193", "a194", "a195", "a196", "a197", "a198", "a199", "a200", "a201", "a202", "a203", "a204", "a205", "a206", "a207", "a208", "a209", "a210", "a211", "a212", "a213", "a214", "a215", "a216", "a217", "a218", "a219", "a220", "a221", "a222", "a223", "a224", "a225", "a226", "a227", "a228", "a229", "a230", "a231", "a232", "a233", "a234", "a235", "a236", "a237", "a238", "a239", "a240", "a241", "a242", "a243", "a244", "a245", "a246", "a247", "a248", "a249", "a250", "a251", "a252", "a253", "a254", "a255", "s33", "s34", "s35", "s36", "s37", "s38", "s39", "s40", "s41", "s42", "s43", "s44", "s45", "s46", "s47", "s48", "s49", "s50", "s51", "s52", "s53", "s54", "s55", "s56", "s57", "s58", "s59", "s60", "s61", "s62", "s63", "s64", "s65", "s66", "s67", "s68", "s69", "s70", "s71", "s72", "s73", "s74", "s75", "s76", "s77", "s78", "s79", "s80", "s81", "s82", "s83", "s84", "s85", "s86", "s87", "s88", "s89", "s90", "s91", "s92", "s93", "s94", "s95", "s96", "s97", "s98", "s99", "vcc", "scc", "memory");
}

extern "C" void kernel_launch(void* const* d_in, const int* in_sizes, int n_in, void* d_out, int out_size, void* d_ws,
                              size_t ws_size, hipStream_t stream) {
  fpn_lateral_conv_kernel<<<256, 256, 0, stream>>>(d_in[0], d_in[1], d_in[2], d_in[3], d_out, d_ws);
}
```
